```python
import torch

def _softmax(x, axis=-1):
    return torch.softmax(x, dim=axis)
def _gelu(x):
    return torch.nn.functional.gelu(x, approximate="tanh")
def _silu(x):
    return torch.nn.functional.silu(x)
def _sigmoid(x):
    return torch.sigmoid(x)
def _layernorm(x, axis=-1, eps=1e-5):
    m = x.mean(dim=axis, keepdim=True)
    v = x.var(dim=axis, keepdim=True, unbiased=False)
    return (x - m) / torch.sqrt(v + eps)
def _scatter_add(tgt, idx, src, axis=0):
    return tgt.clone().index_add_(axis, idx, src)
def _segment_sum(src, seg, n):
    out = torch.zeros((n,) + src.shape[1:], dtype=src.dtype)
    return out.index_add_(0, seg, src)
def _pad(x, pw):
    flat = tuple(v for pair in reversed(pw) for v in pair)
    return torch.nn.functional.pad(x, flat)

kernel_name = 'sequential_preLN__sliding_local_dilated__swiglu_ffn__prompt'

def setup_inputs():

    _w = (lambda a: (lambda t: t.to(torch.bfloat16) if t.is_floating_point() else t)(torch.as_tensor(a)))
    def _A_setup():
        B, S, D = 1, 2048, 448
        return {
            "x": _w((lambda *s: torch.randn(*s, dtype=torch.bfloat16) * 0.05)(B, S, D)),
            "wqkv": _w((lambda *s: torch.randn(*s, dtype=torch.bfloat16) * 0.05)(D, 3 * D)),
            "wo": _w((lambda *s: torch.randn(*s, dtype=torch.bfloat16) * 0.05)(D, D)),
        }
    _A_in = _A_setup()

    def _B_setup():
        B, S, D, F = 1, 2048, 448, 1792
        return {
            "x": _w((lambda *s: torch.randn(*s, dtype=torch.bfloat16) * 0.05)(B, S, D)),
            "w_gate": _w((lambda *s: torch.randn(*s, dtype=torch.bfloat16) * 0.05)(D, F)),
            "w_up": _w((lambda *s: torch.randn(*s, dtype=torch.bfloat16) * 0.05)(D, F)),
            "w_down": _w((lambda *s: torch.randn(*s, dtype=torch.bfloat16) * 0.05)(F, D)),
        }
    _B_in = _B_setup()

    return {
        "A_x": _A_in["x"],
        "A_wqkv": _A_in["wqkv"],
        "A_wo": _A_in["wo"],
        "B_x": _B_in["x"],
        "B_w_gate": _B_in["w_gate"],
        "B_w_up": _B_in["w_up"],
        "B_w_down": _B_in["w_down"],
    }

def reference(A_x, A_wqkv, A_wo, B_x, B_w_gate, B_w_up, B_w_down):
    def _A_ref(x, wqkv, wo):
        B, S, D = x.shape
        H = 16; Dh = D // H
        W, dil = 8, 2
        qkv = (lambda x, *d: x.permute(*d))((x @ wqkv).reshape(B, S, 3, H, Dh), 2, 0, 3, 1, 4)
        q, k, v = qkv[0], qkv[1], qkv[2]
        scale = Dh ** -0.5
        pos = torch.arange(S)
        off = torch.arange(W) * dil
        idx = pos[:, None] - off[None, :]
        mask = idx >= 0
        idx_c = torch.where(mask, idx, 0).reshape(-1)
        kg = k[:, :, idx_c, :].reshape(B, H, S, W, Dh)
        vg = v[:, :, idx_c, :].reshape(B, H, S, W, Dh)
        logits = torch.einsum("bhsd,bhswd->bhsw", q, kg) * scale
        logits = torch.where(mask[None, None, :, :], logits, -1e9)
        attn = _softmax(logits, axis=-1)
        o = torch.einsum("bhsw,bhswd->bhsd", attn, vg)
        return (lambda x, *d: x.permute(*d))(o, 0, 2, 1, 3).reshape(B, S, D) @ wo

    def _B_ref(x, w_gate, w_up, w_down):
        g = _silu(x @ w_gate)
        u = x @ w_up
        return (g * u) @ w_down

    x = A_x
    h = x + _A_ref(_layernorm(x, axis=-1), A_wqkv, A_wo)
    h2 = h + _B_ref(_layernorm(h, axis=-1), B_w_gate, B_w_up, B_w_down)
    return h2
```

```cpp
#include <hip/hip_runtime.h>
#include <hip/hip_bf16.h>
#include <stdint.h>

typedef __attribute__((ext_vector_type(8))) __bf16 bf16x8;
typedef __attribute__((ext_vector_type(2))) __bf16 bf16x2;
typedef __attribute__((ext_vector_type(4))) short s16x4;
typedef __attribute__((ext_vector_type(8))) short s16x8;
typedef __attribute__((ext_vector_type(4))) float f32x4;
typedef __attribute__((ext_vector_type(4))) unsigned u32x4;
typedef __attribute__((ext_vector_type(2))) unsigned u32x2;
typedef unsigned short u16;

#define LDS_AS __attribute__((address_space(3)))

constexpr int S_ = 2048, D_ = 448, F_ = 1792, QKVN = 1344;

__device__ __forceinline__ float bf_lo(unsigned u) { return __uint_as_float(u << 16); }
__device__ __forceinline__ float bf_hi(unsigned u) { return __uint_as_float(u & 0xffff0000u); }
__device__ __forceinline__ unsigned pack_bf16(float a, float b) {
  bf16x2 v; v[0] = (__bf16)a; v[1] = (__bf16)b;
  return __builtin_bit_cast(unsigned, v);
}
__device__ __forceinline__ float rbf(float a) {
  __bf16 b = (__bf16)a; return (float)b;
}
__device__ __forceinline__ float dot2bf(unsigned a, unsigned b, float c) {
  return __builtin_amdgcn_fdot2_f32_bf16(__builtin_bit_cast(bf16x2, a), __builtin_bit_cast(bf16x2, b), c, false);
}
__device__ __forceinline__ void glds16(const void* g, void* l) {
  __builtin_amdgcn_global_load_lds((const unsigned*)g, (unsigned*)l, 16, 0, 0);
}
#define WAIT_VM(n) asm volatile("s_waitcnt vmcnt(" #n ")" ::: "memory")
#define WAIT_LGKM0() asm volatile("s_waitcnt lgkmcnt(0)" ::: "memory")
#define BAR() __builtin_amdgcn_s_barrier()

__device__ __forceinline__ bf16x8 ldB(const char* p) {
  s16x4 a = __builtin_amdgcn_ds_read_tr16_b64_v4i16((LDS_AS s16x4*)(p));
  s16x4 b = __builtin_amdgcn_ds_read_tr16_b64_v4i16((LDS_AS s16x4*)(p + 512));
  s16x8 r = __builtin_shufflevector(a, b, 0, 1, 2, 3, 4, 5, 6, 7);
  return __builtin_bit_cast(bf16x8, r);
}
__device__ __forceinline__ bf16x8 ldA(const char* p) { return *(const bf16x8*)p; }
__device__ __forceinline__ f32x4 mfma(bf16x8 a, bf16x8 b, f32x4 c) {
  return __builtin_amdgcn_mfma_f32_16x16x32_bf16(a, b, c, 0, 0, 0);
}

constexpr int K1_A_BYTES = 7 * 10 * 1024;
constexpr int K1_STAGE = 24 * 1024;
constexpr int K1_LDS = K1_A_BYTES + 3 * K1_STAGE;
constexpr int K1_QP = 392;

__global__ __launch_bounds__(512) void k1_qkv_attn(const u16* __restrict__ x, const u16* __restrict__ wqkv,
                                                   u16* __restrict__ o) {
  extern __shared__ __attribute__((aligned(16))) char smem[];
  char* sA = smem;
  char* sB = smem + K1_A_BYTES;
  const int tid = threadIdx.x, l = tid & 63;
  const int w = __builtin_amdgcn_readfirstlane(tid >> 6);
  const int bid = blockIdx.x;
  const int mblk = (bid & 7) * 4 + ((bid >> 3) & 3), hp = bid >> 5;
  const int m0 = mblk * 64;
  const int r = l >> 3, c = l & 7;

  u32x4 xv[2][7];
#pragma unroll
  for (int ps = 0; ps < 2; ++ps) {
    if (ps == 0 || w < 2) {
      int rg = ps * 8 + w;
      int grow = m0 - 16 + rg * 8 + r; grow = grow < 0 ? 0 : grow;
      const u16* p = x + (size_t)grow * D_ + c * 8;
#pragma unroll
      for (int kb = 0; kb < 7; ++kb) xv[ps][kb] = *(const u32x4*)(p + kb * 64);
    }
  }
  int bcol;
  {
    int tp = c >> 1, half = c & 1;
    int swz = ((r >> 1) & 1) | ((w & 1) << 1);
    bcol = hp * 56 + 16 * (tp ^ swz) + 8 * half;
  }
  auto issueB = [&](int ch, int stage) {
#pragma unroll
    for (int part = 0; part < 3; ++part) {
      int idx = (ch * 64 + w * 8 + r) * QKVN + part * D_ + bcol;
      idx = idx > (D_ * QKVN - 8) ? (D_ * QKVN - 8) : idx;
      glds16(wqkv + idx, sB + stage * K1_STAGE + (part * 8 + w) * 1024 + l * 16);
    }
  };
  issueB(0, 0); issueB(1, 1); issueB(2, 2);

#pragma unroll
  for (int ps = 0; ps < 2; ++ps) {
    if (ps == 0 || w < 2) {
      int rg = ps * 8 + w;
      float f[7][8];
      float s = 0.f;
#pragma unroll
      for (int kb = 0; kb < 7; ++kb)
#pragma unroll
        for (int j = 0; j < 4; ++j) {
          f[kb][2 * j] = bf_lo(xv[ps][kb][j]); f[kb][2 * j + 1] = bf_hi(xv[ps][kb][j]);
          s += f[kb][2 * j] + f[kb][2 * j + 1];
        }
      s += __shfl_xor(s, 1); s += __shfl_xor(s, 2); s += __shfl_xor(s, 4);
      float mean = s * (1.0f / D_);
      float v = 0.f;
#pragma unroll
      for (int kb = 0; kb < 7; ++kb)
#pragma unroll
        for (int j = 0; j < 8; ++j) { f[kb][j] -= mean; v += f[kb][j] * f[kb][j]; }
      v += __shfl_xor(v, 1); v += __shfl_xor(v, 2); v += __shfl_xor(v, 4);
      float rstd = 1.0f / sqrtf(v * (1.0f / D_) + 1e-5f);
      int swzA = (rg & 1) * 4 + (r >> 1);
#pragma unroll
      for (int kb = 0; kb < 7; ++kb) {
        u32x4 pk;
#pragma unroll
        for (int j = 0; j < 4; ++j) pk[j] = pack_bf16(f[kb][2 * j] * rstd, f[kb][2 * j + 1] * rstd);
        *(u32x4*)(sA + (kb * 10 + rg) * 1024 + r * 128 + ((c ^ swzA) * 16)) = pk;
      }
    }
  }

  const int mt = w & 3, nh = w >> 2;
  const int row16 = l & 15, g = l >> 4, q = (l >> 2) & 3, p4 = l & 3;
  const int aoff_own = (2 * (mt + 1) + (row16 >> 3)) * 1024 + (row16 & 7) * 128;
  const int aoff_halo = (row16 >> 3) * 1024 + (row16 & 7) * 128;
  const int aswz = (row16 >> 1) & 7;
  const int boff0 = g * 1024 + q * 128 + ((((q >> 1) & 1) | ((g & 1) << 1)) * 32) + p4 * 8;
  f32x4 acc[6], acch;
#pragma unroll
  for (int i = 0; i < 6; ++i) acc[i] = (f32x4){0.f, 0.f, 0.f, 0.f};
  acch = (f32x4){0.f, 0.f, 0.f, 0.f};
  const int hnt = 4 + w;

#pragma unroll
  for (int ch = 0; ch < 7; ++ch) {
    if (ch <= 4) { WAIT_VM(6); } else if (ch == 5) { WAIT_VM(3); } else { WAIT_VM(0); }
    WAIT_LGKM0();
    BAR();
    const char* stg = sB + (ch % 3) * K1_STAGE;
    const char* aimg = sA + ch * 10 * 1024;
#pragma unroll
    for (int ks2 = 0; ks2 < 2; ++ks2) {
      int cs = ((4 * ks2 + g) ^ aswz) * 16;
      bf16x8 a_own = ldA(aimg + aoff_own + cs);
      bf16x8 a_halo = ldA(aimg + aoff_halo + cs);
#pragma unroll
      for (int i = 0; i < 6; ++i) {
        int nt12 = nh * 6 + i;
        bf16x8 b = ldB(stg + (nt12 >> 2) * 8192 + ks2 * 4096 + (boff0 ^ ((nt12 & 3) << 5)));
        acc[i] = mfma(b, a_own, acc[i]);
      }
      {
        bf16x8 b = ldB(stg + (hnt >> 2) * 8192 + ks2 * 4096 + (boff0 ^ ((hnt & 3) << 5)));
        acch = mfma(b, a_halo, acch);
      }
    }
    WAIT_LGKM0();
    BAR();
    if (ch + 3 < 7) issueB(ch + 3, ch % 3);
  }

  char* sQ = sB;
  {
#pragma unroll
    for (int i = 0; i < 6; ++i) {
      int nt12 = nh * 6 + i;
      u32x2 v2; v2[0] = pack_bf16(acc[i][0], acc[i][1]); v2[1] = pack_bf16(acc[i][2], acc[i][3]);
      *(u32x2*)(sQ + (16 * (mt + 1) + row16) * K1_QP + (nt12 * 16 + 4 * g) * 2) = v2;
    }
    u32x2 v2; v2[0] = pack_bf16(acch[0], acch[1]); v2[1] = pack_bf16(acch[2], acch[3]);
    *(u32x2*)(sQ + row16 * K1_QP + (hnt * 16 + 4 * g) * 2) = v2;
  }
  __syncthreads();

  {
    const int pairi = tid >> 2, j = tid & 3;
    const int t = pairi >> 1, hd = pairi & 1;
    const int row = 16 + t;
    const char* qp = sQ + row * K1_QP + hd * 56;
    unsigned qv[14];
#pragma unroll
    for (int i = 0; i < 7; ++i) { u32x2 v = *(const u32x2*)(qp + i * 8); qv[2 * i] = v[0]; qv[2 * i + 1] = v[1]; }
    float lg[2];
#pragma unroll
    for (int e = 0; e < 2; ++e) {
      int wk = 2 * j + e;
      const char* kp = sQ + (row - 2 * wk) * K1_QP + 128 + hd * 56;
      float d = 0.f;
#pragma unroll
      for (int i = 0; i < 7; ++i) {
        u32x2 v = *(const u32x2*)(kp + i * 8);
        d = dot2bf(qv[2 * i], v[0], d);
        d = dot2bf(qv[2 * i + 1], v[1], d);
      }
      d = rbf(rbf(d) * 0.18898223650461362f);
      lg[e] = (m0 + t - 2 * wk >= 0) ? d : -1e9f;
    }
    float mx = fmaxf(lg[0], lg[1]);
    mx = fmaxf(mx, __shfl_xor(mx, 1)); mx = fmaxf(mx, __shfl_xor(mx, 2));
    float e0 = __expf(lg[0] - mx), e1 = __expf(lg[1] - mx);
    float sm = e0 + e1;
    sm += __shfl_xor(sm, 1); sm += __shfl_xor(sm, 2);
    float inv = 1.0f / sm;
    float p0 = rbf(e0 * inv), p1 = rbf(e1 * inv);
    float oacc[8];
#pragma unroll
    for (int i = 0; i < 8; ++i) oacc[i] = 0.f;
    const int lbase = l & ~3;
#pragma unroll
    for (int wk = 0; wk < 8; ++wk) {
      float pw = __shfl((wk & 1) ? p1 : p0, lbase + (wk >> 1));
      const char* vp = sQ + (row - 2 * wk) * K1_QP + 256 + hd * 56 + j * 16;
      u32x2 va = *(const u32x2*)(vp), vb = *(const u32x2*)(vp + 8);
      oacc[0] += pw * bf_lo(va[0]); oacc[1] += pw * bf_hi(va[0]);
      oacc[2] += pw * bf_lo(va[1]); oacc[3] += pw * bf_hi(va[1]);
      oacc[4] += pw * bf_lo(vb[0]); oacc[5] += pw * bf_hi(vb[0]);
      oacc[6] += pw * bf_lo(vb[1]); oacc[7] += pw * bf_hi(vb[1]);
    }
    u16* op = o + (size_t)(m0 + t) * D_ + hp * 56 + hd * 28 + j * 8;
    u32x2 s0; s0[0] = pack_bf16(oacc[0], oacc[1]); s0[1] = pack_bf16(oacc[2], oacc[3]);
    *(u32x2*)op = s0;
    if (j < 3) {
      u32x2 s1; s1[0] = pack_bf16(oacc[4], oacc[5]); s1[1] = pack_bf16(oacc[6], oacc[7]);
      *(u32x2*)(op + 4) = s1;
    }
  }
}

constexpr int G64_WAVE_LDS = 32 * 1024;
constexpr int G64_LDS = 4 * G64_WAVE_LDS;

template <int NCH, bool STATS>
__global__ __launch_bounds__(256) void gemm64(const u16* __restrict__ A, const u16* __restrict__ B,
                                              const u16* __restrict__ resid, u16* __restrict__ out,
                                              float2* __restrict__ stats) {
  extern __shared__ __attribute__((aligned(16))) char smem[];
  constexpr int LDA = NCH * 64;
  const int tid = threadIdx.x, l = tid & 63;
  const int w = __builtin_amdgcn_readfirstlane(tid >> 6);
  const int bid = blockIdx.x;
  const int loc = bid >> 3;
  const int mblk = (bid & 7) * 4 + (loc & 3), nblk = loc >> 2;
  const int m0 = mblk * 64, n0 = nblk * 64;
  const int r = l >> 3, c = l & 7;
  const int row16 = l & 15, g = l >> 4, q = (l >> 2) & 3, p4 = l & 3;
  char* ws = smem + w * G64_WAVE_LDS;

  u32x2 rv[4];
#pragma unroll
  for (int t = 0; t < 4; ++t)
    rv[t] = *(const u32x2*)(resid + (size_t)(m0 + 16 * w + row16) * D_ + n0 + 16 * t + 4 * g);

  const int tp = c >> 1, half = c & 1;
  auto issue = [&](int ch, int stage) {
    char* dst = ws + stage * 16384;
#pragma unroll
    for (int rg = 0; rg < 8; ++rg) {
      int swzA = (rg & 1) * 4 + (r >> 1);
      glds16(A + (size_t)(m0 + rg * 8 + r) * LDA + ch * 64 + ((c ^ swzA) * 8), dst + rg * 1024 + l * 16);
    }
#pragma unroll
    for (int kb = 0; kb < 8; ++kb) {
      int swz = ((r >> 1) & 1) | ((kb & 1) << 1);
      glds16(B + (size_t)(ch * 64 + kb * 8 + r) * D_ + n0 + 16 * (tp ^ swz) + 8 * half, dst + 8192 + kb * 1024 + l * 16);
    }
  };
  const int nmine = (NCH - w + 3) / 4;
  issue(w, 0);
  if (nmine > 1) issue(w + 4, 1);

  const int aoff = (row16 >> 3) * 1024 + (row16 & 7) * 128;
  const int aswz = (row16 >> 1) & 7;
  const int boff0 = g * 1024 + q * 128 + ((((q >> 1) & 1) | ((g & 1) << 1)) * 32) + p4 * 8;
  f32x4 acc[4][4];
#pragma unroll
  for (int i = 0; i < 4; ++i)
#pragma unroll
    for (int j = 0; j < 4; ++j) acc[i][j] = (f32x4){0.f, 0.f, 0.f, 0.f};

  for (int i = 0; i < nmine; ++i) {
    if (i + 1 < nmine) { WAIT_VM(16); } else { WAIT_VM(0); }
    const char* st = ws + (i & 1) * 16384;
#pragma unroll
    for (int ks2 = 0; ks2 < 2; ++ks2) {
      int cs = ((4 * ks2 + g) ^ aswz) * 16;
      bf16x8 a[4], b[4];
#pragma unroll
      for (int m = 0; m < 4; ++m) a[m] = ldA(st + m * 2048 + aoff + cs);
#pragma unroll
      for (int t = 0; t < 4; ++t) b[t] = ldB(st + 8192 + ks2 * 4096 + (boff0 ^ (t << 5)));
#pragma unroll
      for (int m = 0; m < 4; ++m)
#pragma unroll
        for (int t = 0; t < 4; ++t) acc[m][t] = mfma(b[t], a[m], acc[m][t]);
    }
    WAIT_LGKM0();
    if (i + 2 < nmine) issue(w + 4 * (i + 2), i & 1);
  }
  __syncthreads();
  {
    f32x4* red = (f32x4*)smem;
#pragma unroll
    for (int m = 0; m < 4; ++m)
#pragma unroll
      for (int t = 0; t < 4; ++t) red[((w * 4 + m) * 4 + t) * 64 + l] = acc[m][t];
  }
  __syncthreads();
  float hv[4][4];
  {
    const f32x4* red = (const f32x4*)smem;
#pragma unroll
    for (int t = 0; t < 4; ++t) {
      f32x4 s = red[((0 * 4 + w) * 4 + t) * 64 + l];
#pragma unroll
      for (int ww = 1; ww < 4; ++ww) s += red[((ww * 4 + w) * 4 + t) * 64 + l];
      float r0 = bf_lo(rv[t][0]), r1 = bf_hi(rv[t][0]), r2 = bf_lo(rv[t][1]), r3 = bf_hi(rv[t][1]);
      hv[t][0] = rbf(r0 + rbf(s[0])); hv[t][1] = rbf(r1 + rbf(s[1]));
      hv[t][2] = rbf(r2 + rbf(s[2])); hv[t][3] = rbf(r3 + rbf(s[3]));
      u32x2 o2; o2[0] = pack_bf16(hv[t][0], hv[t][1]); o2[1] = pack_bf16(hv[t][2], hv[t][3]);
      *(u32x2*)(out + (size_t)(m0 + 16 * w + row16) * D_ + n0 + 16 * t + 4 * g) = o2;
    }
  }
  if (STATS) {
    float s = 0.f;
#pragma unroll
    for (int t = 0; t < 4; ++t) s += hv[t][0] + hv[t][1] + hv[t][2] + hv[t][3];
    s += __shfl_xor(s, 16); s += __shfl_xor(s, 32);
    float mean = s * (1.0f / 64.0f), m2 = 0.f;
#pragma unroll
    for (int t = 0; t < 4; ++t)
#pragma unroll
      for (int j = 0; j < 4; ++j) { float d = hv[t][j] - mean; m2 += d * d; }
    m2 += __shfl_xor(m2, 16); m2 += __shfl_xor(m2, 32);
    if (g == 0) stats[(size_t)(m0 + 16 * w + row16) * 7 + nblk] = make_float2(s, m2);
  }
}

constexpr int K3_STAGE = 32 * 1024;
constexpr int K3_LDS = 4 * K3_STAGE + 4 * 8192;

__global__ __launch_bounds__(256) void k3_ffn1(const u16* __restrict__ h, const float2* __restrict__ stats,
                                               const u16* __restrict__ wg, const u16* __restrict__ wu,
                                               u16* __restrict__ act) {
  extern __shared__ __attribute__((aligned(16))) char smem[];
  const int tid = threadIdx.x, l = tid & 63;
  const int w = __builtin_amdgcn_readfirstlane(tid >> 6);
  const int bid = blockIdx.x;
  const int xcd = bid & 7, loc = bid >> 3;
  const int mblk = (xcd >> 1) * 4 + (loc & 3), fblk = (xcd & 1) * 7 + (loc >> 2);
  const int m0 = mblk * 128, f0 = fblk * 128;
  const int r = l >> 3, c = l & 7;
  char* sB = smem;
  char* scr = smem + 4 * K3_STAGE + w * 8192;

  u32x4 hv[4][7];
  float2 st[4];
#pragma unroll
  for (int ps = 0; ps < 4; ++ps) {
    int row = m0 + 32 * w + ps * 8 + r;
    const u16* p = h + (size_t)row * D_ + c * 8;
#pragma unroll
    for (int kb = 0; kb < 7; ++kb) hv[ps][kb] = *(const u32x4*)(p + kb * 64);
    st[ps] = stats[(size_t)row * 7 + (c < 7 ? c : 6)];
  }
  const int tp = c >> 1, half = c & 1;
  auto issueB = [&](int ch, int stage) {
#pragma unroll
    for (int i = 0; i < 8; ++i) {
      int kb = w + 4 * (i & 1), mc = i >> 1;
      const u16* W = (mc >> 1) ? wu : wg;
      int swz = ((r >> 1) & 1) | ((kb & 1) << 1);
      glds16(W + (size_t)(ch * 64 + kb * 8 + r) * F_ + f0 + (mc & 1) * 64 + 16 * (tp ^ swz) + 8 * half,
             sB + stage * K3_STAGE + mc * 8192 + kb * 1024 + l * 16);
    }
  };
  issueB(0, 0); issueB(1, 1); issueB(2, 2); issueB(3, 3);

  const int row16 = l & 15, g = l >> 4, q = (l >> 2) & 3, p4 = l & 3;
  const int aoff = (row16 >> 3) * 1024 + (row16 & 7) * 128;
  const int aswz = (row16 >> 1) & 7;
  bf16x8 af[2][14];
  float mean_[4], rstd_[4];
#pragma unroll
  for (int ps = 0; ps < 4; ++ps) {
    float s = (c < 7) ? st[ps].x : 0.f;
    float tot = s;
    tot += __shfl_xor(tot, 1); tot += __shfl_xor(tot, 2); tot += __shfl_xor(tot, 4);
    float mean = tot * (1.0f / D_);
    float dm = s * (1.0f / 64.0f) - mean;
    float m2 = (c < 7) ? (st[ps].y + 64.0f * dm * dm) : 0.f;
    m2 += __shfl_xor(m2, 1); m2 += __shfl_xor(m2, 2); m2 += __shfl_xor(m2, 4);
    mean_[ps] = mean;
    rstd_[ps] = 1.0f / sqrtf(m2 * (1.0f / D_) + 1e-5f);
  }
#pragma unroll
  for (int m = 0; m < 2; ++m) {
#pragma unroll
    for (int kg = 0; kg < 2; ++kg) {
      const int nk = kg == 0 ? 4 : 3;
#pragma unroll
      for (int hrg = 0; hrg < 2; ++hrg) {
        int ps = 2 * m + hrg;
        int swzA = hrg * 4 + (r >> 1);
        float mu = mean_[ps], rs = rstd_[ps];
#pragma unroll
        for (int kk = 0; kk < nk; ++kk) {
          int kb = kg * 4 + kk;
          u32x4 pk;
#pragma unroll
          for (int j = 0; j < 4; ++j)
            pk[j] = pack_bf16((bf_lo(hv[ps][kb][j]) - mu) * rs, (bf_hi(hv[ps][kb][j]) - mu) * rs);
          *(u32x4*)(scr + (kk * 2 + hrg) * 1024 + r * 128 + ((c ^ swzA) * 16)) = pk;
        }
      }
#pragma unroll
      for (int kk = 0; kk < nk; ++kk)
#pragma unroll
        for (int ks2 = 0; ks2 < 2; ++ks2)
          af[m][(kg * 4 + kk) * 2 + ks2] = ldA(scr + kk * 2048 + aoff + (((4 * ks2 + g) ^ aswz) * 16));
    }
  }

  const int boff0 = g * 1024 + q * 128 + ((((q >> 1) & 1) | ((g & 1) << 1)) * 32) + p4 * 8;
  f32x4 acc[2][4][4];
#pragma unroll
  for (int m = 0; m < 2; ++m)
#pragma unroll
    for (int i = 0; i < 4; ++i)
#pragma unroll
      for (int j = 0; j < 4; ++j) acc[m][i][j] = (f32x4){0.f, 0.f, 0.f, 0.f};

#pragma unroll
  for (int ch = 0; ch < 7; ++ch) {
    if (ch <= 3) { WAIT_VM(24); } else if (ch == 4) { WAIT_VM(16); } else if (ch == 5) { WAIT_VM(8); } else { WAIT_VM(0); }
    WAIT_LGKM0();
    BAR();
    const char* stg = sB + (ch & 3) * K3_STAGE;
#pragma unroll
    for (int ks2 = 0; ks2 < 2; ++ks2) {
#pragma unroll
      for (int mc = 0; mc < 4; ++mc)
#pragma unroll
        for (int t = 0; t < 4; ++t) {
          bf16x8 b = ldB(stg + mc * 8192 + ks2 * 4096 + (boff0 ^ (t << 5)));
          acc[0][mc][t] = mfma(b, af[0][ch * 2 + ks2], acc[0][mc][t]);
          acc[1][mc][t] = mfma(b, af[1][ch * 2 + ks2], acc[1][mc][t]);
        }
    }
    if (ch + 4 < 7) {
      WAIT_LGKM0();
      BAR();
      issueB(ch + 4, ch & 3);
    }
  }
#pragma unroll
  for (int m = 0; m < 2; ++m)
#pragma unroll
    for (int cb = 0; cb < 2; ++cb)
#pragma unroll
      for (int t = 0; t < 4; ++t) {
        float a4[4];
#pragma unroll
        for (int j = 0; j < 4; ++j) {
          float gv = rbf(acc[m][cb][t][j]);
          float uv = rbf(acc[m][2 + cb][t][j]);
          float sg = rbf(gv / (1.0f + __expf(-gv)));
          a4[j] = sg * uv;
        }
        u32x2 o2; o2[0] = pack_bf16(a4[0], a4[1]); o2[1] = pack_bf16(a4[2], a4[3]);
        *(u32x2*)(act + (size_t)(m0 + 32 * w + 16 * m + row16) * F_ + f0 + cb * 64 + 16 * t + 4 * g) = o2;
      }
}

extern "C" void kernel_launch(void* const* d_in, const int* in_sizes, int n_in, void* d_out, int out_size,
                              void* d_ws, size_t ws_size, hipStream_t stream) {
  const u16* x = (const u16*)d_in[0];
  const u16* wqkv = (const u16*)d_in[1];
  const u16* wo = (const u16*)d_in[2];
  const u16* wgate = (const u16*)d_in[4];
  const u16* wup = (const u16*)d_in[5];
  const u16* wdown = (const u16*)d_in[6];
  u16* out = (u16*)d_out;
  char* ws = (char*)d_ws;
  u16* o = (u16*)ws;
  u16* h = (u16*)(ws + (2u << 20));
  float2* stats = (float2*)(ws + (4u << 20));
  u16* act = (u16*)(ws + (8u << 20));

  hipFuncSetAttribute((const void*)k1_qkv_attn, hipFuncAttributeMaxDynamicSharedMemorySize, K1_LDS);
  hipFuncSetAttribute((const void*)gemm64<7, true>, hipFuncAttributeMaxDynamicSharedMemorySize, G64_LDS);
  hipFuncSetAttribute((const void*)gemm64<28, false>, hipFuncAttributeMaxDynamicSharedMemorySize, G64_LDS);
  hipFuncSetAttribute((const void*)k3_ffn1, hipFuncAttributeMaxDynamicSharedMemorySize, K3_LDS);

  k1_qkv_attn<<<256, 512, K1_LDS, stream>>>(x, wqkv, o);
  gemm64<7, true><<<224, 256, G64_LDS, stream>>>(o, wo, x, h, stats);
  k3_ffn1<<<224, 256, K3_LDS, stream>>>(h, stats, wgate, wup, act);
  gemm64<28, false><<<224, 256, G64_LDS, stream>>>(act, wdown, h, out, nullptr);
}
```

```cpp
#include <hip/hip_runtime.h>
#include <hip/hip_bf16.h>
#include <stdint.h>

typedef __attribute__((ext_vector_type(8))) __bf16 bf16x8;
typedef __attribute__((ext_vector_type(2))) __bf16 bf16x2;
typedef __attribute__((ext_vector_type(4))) short s16x4;
typedef __attribute__((ext_vector_type(8))) short s16x8;
typedef __attribute__((ext_vector_type(4))) float f32x4;
typedef __attribute__((ext_vector_type(4))) unsigned u32x4;
typedef __attribute__((ext_vector_type(2))) unsigned u32x2;
typedef unsigned short u16;

#define LDS_AS __attribute__((address_space(3)))

constexpr int S_ = 2048, D_ = 448, F_ = 1792, QKVN = 1344;

__device__ __forceinline__ float bf_lo(unsigned u) { return __uint_as_float(u << 16); }
__device__ __forceinline__ float bf_hi(unsigned u) { return __uint_as_float(u & 0xffff0000u); }
__device__ __forceinline__ unsigned pack_bf16(float a, float b) {
  bf16x2 v; v[0] = (__bf16)a; v[1] = (__bf16)b;
  return __builtin_bit_cast(unsigned, v);
}
__device__ __forceinline__ float rbf(float a) {
  __bf16 b = (__bf16)a; return (float)b;
}
__device__ __forceinline__ float dot2bf(unsigned a, unsigned b, float c) {
  return __builtin_amdgcn_fdot2_f32_bf16(__builtin_bit_cast(bf16x2, a), __builtin_bit_cast(bf16x2, b), c, false);
}
__device__ __forceinline__ void glds16(const void* sbase, unsigned voff, unsigned lds_addr) {
  unsigned keep;
  asm volatile("s_mov_b32 %0, m0\n\ts_mov_b32 m0, %3\n\ts_nop 0\n\tglobal_load_lds_dwordx4 %1, %2\n\ts_mov_b32 m0, %0"
               : "=&s"(keep) : "v"(voff), "s"(sbase), "s"(lds_addr) : "memory");
}
template <int IMM>
__device__ __forceinline__ u32x4 gload16(const void* sbase, unsigned voff) {
  u32x4 r;
  asm volatile("global_load_dwordx4 %0, %1, %2 offset:%3" : "=v"(r) : "v"(voff), "s"(sbase), "n"(IMM) : "memory");
  return r;
}
template <int IMM>
__device__ __forceinline__ u32x2 gload8(const void* sbase, unsigned voff) {
  u32x2 r;
  asm volatile("global_load_dwordx2 %0, %1, %2 offset:%3" : "=v"(r) : "v"(voff), "s"(sbase), "n"(IMM) : "memory");
  return r;
}
__device__ __forceinline__ unsigned lds_off(const void* p) { return (unsigned)(uintptr_t)(LDS_AS const char*)p; }
#define PIN(x) asm volatile("" : "+v"(x))
#define STAMP(kid, i)
#define WAIT_VMN(n) asm volatile("s_waitcnt vmcnt(%0)" :: "n"(n) : "memory")
#define WAIT_LGKM0() asm volatile("s_waitcnt lgkmcnt(0)" ::: "memory")
#define BAR() __builtin_amdgcn_s_barrier()

__device__ __forceinline__ bf16x8 ldB(const char* p) {
  s16x4 a = __builtin_amdgcn_ds_read_tr16_b64_v4i16((LDS_AS s16x4*)(p));
  s16x4 b = __builtin_amdgcn_ds_read_tr16_b64_v4i16((LDS_AS s16x4*)(p + 512));
  s16x8 r = __builtin_shufflevector(a, b, 0, 1, 2, 3, 4, 5, 6, 7);
  return __builtin_bit_cast(bf16x8, r);
}
__device__ __forceinline__ u32x4 ldA(const char* p) { return *(const u32x4*)p; }
__device__ __forceinline__ f32x4 mfma(bf16x8 a, u32x4 b, f32x4 c) {
  return __builtin_amdgcn_mfma_f32_16x16x32_bf16(a, __builtin_bit_cast(bf16x8, b), c, 0, 0, 0);
}
constexpr int cmin(int a, int b) { return a < b ? a : b; }

constexpr int K1_STAGE = 34 * 1024;
constexpr int K1_NST = 4;
constexpr int K1_LDS = K1_NST * K1_STAGE + 1024;
constexpr int K1_QP = 392;

__global__ __launch_bounds__(768) void k1_qkv_attn(const u16* __restrict__ x, const u16* __restrict__ wqkv,
                                                   u16* __restrict__ o, const u16* __restrict__ wo,
                                                   const u16* __restrict__ wg, const u16* __restrict__ wu,
                                                   const u16* __restrict__ wd) {
  extern __shared__ __attribute__((aligned(16))) char smem[];
  float* sCS = (float*)(smem + K1_NST * K1_STAGE);
  const int tid = threadIdx.x, l = tid & 63;
  const int w = __builtin_amdgcn_readfirstlane(tid >> 6);
  const int bid = blockIdx.x;
  const int mblk = (bid & 7) * 4 + ((bid >> 3) & 3), hp = bid >> 5;
  const int m0 = mblk * 64;
  const int r = l >> 3, c = l & 7;
  STAMP(0, 0);
  if (w >= 8) {
    const int lw = w - 8;
    const unsigned s_off = lds_off(smem);
    const int asw = (c ^ ((lw & 1) * 4 + (r >> 1))) * 8;
    int grow = m0 - 16 + lw * 8 + r; grow = grow < 0 ? 0 : grow;
    const unsigned avo0 = (unsigned)(grow * D_ + asw) * 2u;
    const unsigned avo1 = (unsigned)((m0 + 16 + lw * 8 + r) * D_ + asw) * 2u;
    const unsigned avo2 = (unsigned)((m0 + 48 + lw * 8 + r) * D_ + asw) * 2u;
    const int tp = c >> 1, half = c & 1;
    const unsigned bvoff = (unsigned)((lw * 8 + r) * QKVN + hp * 56 + 16 * (tp ^ (((r >> 1) & 1) | ((lw & 1) << 1))) + 8 * half) * 2u;
    auto issue = [&](int ch) {
      unsigned dst = s_off + (ch % K1_NST) * K1_STAGE;
      glds16(x + ch * 64, avo0, dst + lw * 1024);
      glds16(x + ch * 64, avo1, dst + (4 + lw) * 1024);
      if (lw < 2) glds16(x + ch * 64, avo2, dst + (8 + lw) * 1024);
#pragma unroll
      for (int part = 0; part < 3; ++part)
#pragma unroll
        for (int hk = 0; hk < 2; ++hk) {
          unsigned vo = bvoff + (unsigned)((ch * 64 + hk * 32) * QKVN + part * D_) * 2u;
          vo = vo > (unsigned)(D_ * QKVN - 8) * 2u ? (unsigned)(D_ * QKVN - 8) * 2u : vo;
          glds16(wqkv, vo, dst + (10 + part * 8 + hk * 4 + lw) * 1024);
        }
    };
#pragma unroll
    for (int ch = 0; ch < K1_NST - 1; ++ch) issue(ch);
#pragma unroll
    for (int ch = 0; ch < 7; ++ch) {
      const int ahead = cmin(K1_NST - 2, 6 - ch);
      if (lw < 2) { if (ahead == 2) WAIT_VMN(18); else if (ahead == 1) WAIT_VMN(9); else WAIT_VMN(0); }
      else { if (ahead == 2) WAIT_VMN(16); else if (ahead == 1) WAIT_VMN(8); else WAIT_VMN(0); }
      BAR();
      if (ch + K1_NST - 1 < 7) issue(ch + K1_NST - 1);
    }
    return;
  }

  u32x4 pf[3];
#pragma unroll
  for (int i = 0; i < 3; ++i) {
    int u = bid * 20 + w + 8 * i;
    if (w + 8 * i >= 20 || u >= 5096) u = 0;
    const u16* base = u < 392 ? wo : (u < 1960 ? wg : (u < 3528 ? wu : wd));
    int ub = u < 392 ? u : (u < 1960 ? u - 392 : (u < 3528 ? u - 1960 : u - 3528));
    pf[i] = gload16<0>(base, (unsigned)(ub * 1024 + l * 16));
  }
  const int mt = w & 3, nh = w >> 2;
  const int row16 = l & 15, g = l >> 4, q = (l >> 2) & 3, p4 = l & 3;
  const int aoff_own = (2 * (mt + 1) + (row16 >> 3)) * 1024 + (row16 & 7) * 128;
  const int aoff_halo = (row16 >> 3) * 1024 + (row16 & 7) * 128;
  const int aswz = (row16 >> 1) & 7;
  const int boff0 = 10240 + g * 1024 + q * 128 + ((((q >> 1) & 1) | ((g & 1) << 1)) * 32) + p4 * 8;
  f32x4 acc[6], acch, acchq;
#pragma unroll
  for (int i = 0; i < 6; ++i) acc[i] = (f32x4){0.f, 0.f, 0.f, 0.f};
  acch = (f32x4){0.f, 0.f, 0.f, 0.f}; acchq = acch;
  const int hnt = 4 + w;
  float s1 = 0.f, s2 = 0.f, h1 = 0.f, h2 = 0.f;
  const unsigned ONES = 0x3f803f80u;

#pragma unroll 1
  for (int ch = 0; ch < 7; ++ch) {
    STAMP(0, 2 + 2 * ch);
    WAIT_LGKM0();
    BAR();
    STAMP(0, 3 + 2 * ch);
    const char* stg = smem + (ch % K1_NST) * K1_STAGE;
#pragma unroll
    for (int ks2 = 0; ks2 < 2; ++ks2) {
      int cs = ((4 * ks2 + g) ^ aswz) * 16;
      u32x4 a_own = ldA(stg + aoff_own + cs);
      u32x4 a_halo = ldA(stg + aoff_halo + cs);
#pragma unroll
      for (int j = 0; j < 4; ++j) {
        s1 = dot2bf(a_own[j], ONES, s1); s2 = dot2bf(a_own[j], a_own[j], s2);
        h1 = dot2bf(a_halo[j], ONES, h1); h2 = dot2bf(a_halo[j], a_halo[j], h2);
        a_halo[j] = row16 == 0 ? ONES : a_halo[j];
      }
#pragma unroll
      for (int i = 0; i < 6; ++i) {
        int nt12 = nh * 6 + i;
        bf16x8 b = ldB(stg + (nt12 >> 2) * 8192 + ks2 * 4096 + (boff0 ^ ((nt12 & 3) << 5)));
        acc[i] = mfma(b, a_own, acc[i]);
      }
      {
        bf16x8 b = ldB(stg + (hnt >> 2) * 8192 + ks2 * 4096 + (boff0 ^ ((hnt & 3) << 5)));
        acch = mfma(b, a_halo, acch);
      }
      if (w < 4) {
        bf16x8 b = ldB(stg + ks2 * 4096 + (boff0 ^ (w << 5)));
        acchq = mfma(b, a_halo, acchq);
      }
      __builtin_amdgcn_sched_barrier(0);
    }
  }
  STAMP(0, 16);
  WAIT_VMN(0);
  PIN(pf[0]); PIN(pf[1]); PIN(pf[2]);
  s1 += __shfl_xor(s1, 16); s1 += __shfl_xor(s1, 32);
  s2 += __shfl_xor(s2, 16); s2 += __shfl_xor(s2, 32);
  h1 += __shfl_xor(h1, 16); h1 += __shfl_xor(h1, 32);
  h2 += __shfl_xor(h2, 16); h2 += __shfl_xor(h2, 32);
  const float mean = s1 * (1.0f / D_), hmean = h1 * (1.0f / D_);
  const float rstd = 1.0f / sqrtf(fmaxf(s2 * (1.0f / D_) - mean * mean, 0.f) + 1e-5f);
  const float hrstd = 1.0f / sqrtf(fmaxf(h2 * (1.0f / D_) - hmean * hmean, 0.f) + 1e-5f);
  if (row16 == 0) {
    *(f32x4*)(sCS + hnt * 16 + 4 * g) = acch;
    if (w < 4) *(f32x4*)(sCS + w * 16 + 4 * g) = acchq;
  }
  __syncthreads();

  char* sQ = smem;
  {
#pragma unroll
    for (int i = 0; i < 6; ++i) {
      int nt12 = nh * 6 + i;
      f32x4 cs = *(const f32x4*)(sCS + nt12 * 16 + 4 * g);
      u32x2 v2;
      v2[0] = pack_bf16((acc[i][0] - mean * cs[0]) * rstd, (acc[i][1] - mean * cs[1]) * rstd);
      v2[1] = pack_bf16((acc[i][2] - mean * cs[2]) * rstd, (acc[i][3] - mean * cs[3]) * rstd);
      *(u32x2*)(sQ + (16 * (mt + 1) + row16) * K1_QP + (nt12 * 16 + 4 * g) * 2) = v2;
    }
    f32x4 cs = *(const f32x4*)(sCS + hnt * 16 + 4 * g);
    u32x2 v2;
    v2[0] = pack_bf16((acch[0] - hmean * cs[0]) * hrstd, (acch[1] - hmean * cs[1]) * hrstd);
    v2[1] = pack_bf16((acch[2] - hmean * cs[2]) * hrstd, (acch[3] - hmean * cs[3]) * hrstd);
    *(u32x2*)(sQ + row16 * K1_QP + (hnt * 16 + 4 * g) * 2) = v2;
  }
  __syncthreads();

  {
    const int pairi = tid >> 2, j = tid & 3;
    const int t = pairi >> 1, hd = pairi & 1;
    const int row = 16 + t;
    const char* qp = sQ + row * K1_QP + hd * 56;
    unsigned qv[14];
#pragma unroll
    for (int i = 0; i < 7; ++i) { u32x2 v = *(const u32x2*)(qp + i * 8); qv[2 * i] = v[0]; qv[2 * i + 1] = v[1]; }
    float lg[2];
#pragma unroll
    for (int e = 0; e < 2; ++e) {
      int wk = 2 * j + e;
      const char* kp = sQ + (row - 2 * wk) * K1_QP + 128 + hd * 56;
      float d = 0.f;
#pragma unroll
      for (int i = 0; i < 7; ++i) {
        u32x2 v = *(const u32x2*)(kp + i * 8);
        d = dot2bf(qv[2 * i], v[0], d);
        d = dot2bf(qv[2 * i + 1], v[1], d);
      }
      d = rbf(rbf(d) * 0.18898223650461362f);
      lg[e] = (m0 + t - 2 * wk >= 0) ? d : -1e9f;
    }
    float mx = fmaxf(lg[0], lg[1]);
    mx = fmaxf(mx, __shfl_xor(mx, 1)); mx = fmaxf(mx, __shfl_xor(mx, 2));
    float e0 = __expf(lg[0] - mx), e1 = __expf(lg[1] - mx);
    float sm = e0 + e1;
    sm += __shfl_xor(sm, 1); sm += __shfl_xor(sm, 2);
    float inv = 1.0f / sm;
    float p0 = rbf(e0 * inv), p1 = rbf(e1 * inv);
    float oacc[8];
#pragma unroll
    for (int i = 0; i < 8; ++i) oacc[i] = 0.f;
    const int lbase = l & ~3;
#pragma unroll
    for (int wk = 0; wk < 8; ++wk) {
      float pw = __shfl((wk & 1) ? p1 : p0, lbase + (wk >> 1));
      const char* vp = sQ + (row - 2 * wk) * K1_QP + 256 + hd * 56 + j * 16;
      u32x2 va = *(const u32x2*)(vp), vb = *(const u32x2*)(vp + 8);
      oacc[0] += pw * bf_lo(va[0]); oacc[1] += pw * bf_hi(va[0]);
      oacc[2] += pw * bf_lo(va[1]); oacc[3] += pw * bf_hi(va[1]);
      oacc[4] += pw * bf_lo(vb[0]); oacc[5] += pw * bf_hi(vb[0]);
      oacc[6] += pw * bf_lo(vb[1]); oacc[7] += pw * bf_hi(vb[1]);
    }
    u16* op = o + (size_t)(m0 + t) * D_ + hp * 56 + hd * 28 + j * 8;
    u32x2 s0; s0[0] = pack_bf16(oacc[0], oacc[1]); s0[1] = pack_bf16(oacc[2], oacc[3]);
    *(u32x2*)op = s0;
    if (j < 3) {
      u32x2 s1v; s1v[0] = pack_bf16(oacc[4], oacc[5]); s1v[1] = pack_bf16(oacc[6], oacc[7]);
      *(u32x2*)(op + 4) = s1v;
    }
  }
  STAMP(0, 17);
}

template <int NCH, int NST, int SUB, bool STATS>
__global__ __launch_bounds__(768) void gemm64(const u16* __restrict__ A, const u16* __restrict__ B,
                                              const u16* __restrict__ resid, u16* __restrict__ out,
                                              float2* __restrict__ stats, const u16* __restrict__ wg,
                                              const u16* __restrict__ wu, float* __restrict__ colsum) {
  extern __shared__ __attribute__((aligned(16))) char smem[];
  constexpr int LDA = NCH * SUB * 64;
  constexpr int STG = SUB * 16384;
  const int tid = threadIdx.x, l = tid & 63;
  const int w = __builtin_amdgcn_readfirstlane(tid >> 6);
  const int bid = blockIdx.x;
  if (STATS && bid >= 224) {
    const int j = bid - 224;
    if (j < 28 && tid < 512) {
      const u16* W = (j < 14 ? wg : wu) + (j % 14) * 128;
      const int cc = tid & 15, rs = tid >> 4;
      float a[8];
#pragma unroll
      for (int i = 0; i < 8; ++i) a[i] = 0.f;
#pragma unroll
      for (int i = 0; i < 14; ++i) {
        u32x4 v = *(const u32x4*)(W + (size_t)(rs + 32 * i) * F_ + cc * 8);
#pragma unroll
        for (int e = 0; e < 4; ++e) { a[2 * e] += bf_lo(v[e]); a[2 * e + 1] += bf_hi(v[e]); }
      }
      float* red = (float*)smem;
#pragma unroll
      for (int i = 0; i < 8; ++i) red[rs * 128 + cc * 8 + i] = a[i];
      __syncthreads();
      if (tid < 128) {
        float s = 0.f;
#pragma unroll
        for (int i = 0; i < 32; ++i) s += red[i * 128 + tid];
        colsum[j * 128 + tid] = s;
      }
    }
    return;
  }
  const int loc = bid >> 3;
  const int mblk = (bid & 7) * 4 + (loc & 3), nblk = loc >> 2;
  const int m0 = mblk * 64, n0 = nblk * 64;
  const int r = l >> 3, c = l & 7;
  const int row16 = l & 15, g = l >> 4, q = (l >> 2) & 3, p4 = l & 3;
  const int mt = w & 3, nh = w >> 2;
  STAMP(STATS ? 1 : 3, 0);

  if (w >= 8) {
    const int lw = w - 8;
    const int tp = c >> 1, half = c & 1;
    const unsigned s_off = lds_off(smem);
    const unsigned avo = (unsigned)((m0 + lw * 8 + r) * LDA + ((c ^ ((lw & 1) * 4 + (r >> 1))) * 8)) * 2u;
    const unsigned bvo = (unsigned)((lw * 8 + r) * D_ + n0 + 16 * (tp ^ (((r >> 1) & 1) | ((lw & 1) << 1))) + 8 * half) * 2u;
    auto issue = [&](int ch) {
#pragma unroll
      for (int sub = 0; sub < SUB; ++sub) {
        unsigned dst = s_off + (ch % NST) * STG + sub * 16384;
        const int k0 = (ch * SUB + sub) * 64;
        glds16(A + k0, avo, dst + lw * 1024);
        glds16(A + k0 + 32 * LDA, avo, dst + (4 + lw) * 1024);
        glds16(B + (size_t)k0 * D_, bvo, dst + 8192 + lw * 1024);
        glds16(B + (size_t)(k0 + 32) * D_, bvo, dst + 8192 + (4 + lw) * 1024);
      }
    };
#pragma unroll
    for (int ch = 0; ch < cmin(NST - 1, NCH); ++ch) issue(ch);
#pragma unroll
    for (int ch = 0; ch < NCH; ++ch) {
      WAIT_VMN(4 * SUB * cmin(NST - 2, NCH - 1 - ch));
      BAR();
      if (ch + NST - 1 < NCH) issue(ch + NST - 1);
    }
    return;
  }
  u32x2 rv[2];
  {
    unsigned voff = (unsigned)((m0 + 16 * mt + row16) * D_ + n0 + nh * 32 + 4 * g) * 2u;
    rv[0] = gload8<0>(resid, voff); rv[1] = gload8<32>(resid, voff);
  }
  const int aoff = (2 * mt + (row16 >> 3)) * 1024 + (row16 & 7) * 128;
  const int aswz = (row16 >> 1) & 7;
  const int boff0 = 8192 + g * 1024 + q * 128 + ((((q >> 1) & 1) | ((g & 1) << 1)) * 32) + p4 * 8;
  f32x4 acc[2];
  acc[0] = (f32x4){0.f, 0.f, 0.f, 0.f}; acc[1] = acc[0];

#pragma unroll 1
  for (int ch = 0; ch < NCH; ++ch) {
    WAIT_LGKM0();
    BAR();
    const char* st = smem + (ch % NST) * STG;
#pragma unroll
    for (int sub = 0; sub < SUB; ++sub)
#pragma unroll
      for (int ks2 = 0; ks2 < 2; ++ks2) {
        u32x4 a = ldA(st + sub * 16384 + aoff + (((4 * ks2 + g) ^ aswz) * 16));
#pragma unroll
        for (int t = 0; t < 2; ++t) {
          bf16x8 b = ldB(st + sub * 16384 + ks2 * 4096 + (boff0 ^ ((2 * nh + t) << 5)));
          acc[t] = mfma(b, a, acc[t]);
        }
      }
  }
  WAIT_VMN(0);
  STAMP(STATS ? 1 : 3, 16);
  PIN(rv[0]); PIN(rv[1]);
  float hv[2][4];
#pragma unroll
  for (int t = 0; t < 2; ++t) {
    hv[t][0] = rbf(bf_lo(rv[t][0]) + rbf(acc[t][0])); hv[t][1] = rbf(bf_hi(rv[t][0]) + rbf(acc[t][1]));
    hv[t][2] = rbf(bf_lo(rv[t][1]) + rbf(acc[t][2])); hv[t][3] = rbf(bf_hi(rv[t][1]) + rbf(acc[t][3]));
    u32x2 o2; o2[0] = pack_bf16(hv[t][0], hv[t][1]); o2[1] = pack_bf16(hv[t][2], hv[t][3]);
    *(u32x2*)(out + (size_t)(m0 + 16 * mt + row16) * D_ + n0 + nh * 32 + 16 * t + 4 * g) = o2;
  }
  if (STATS) {
    float s = 0.f;
#pragma unroll
    for (int t = 0; t < 2; ++t) s += hv[t][0] + hv[t][1] + hv[t][2] + hv[t][3];
    s += __shfl_xor(s, 16); s += __shfl_xor(s, 32);
    float mean = s * (1.0f / 32.0f), m2 = 0.f;
#pragma unroll
    for (int t = 0; t < 2; ++t)
#pragma unroll
      for (int j = 0; j < 4; ++j) { float d = hv[t][j] - mean; m2 += d * d; }
    m2 += __shfl_xor(m2, 16); m2 += __shfl_xor(m2, 32);
    if (g == 0) stats[(size_t)(m0 + 16 * mt + row16) * 14 + nblk * 2 + nh] = make_float2(s, m2);
  }
  STAMP(STATS ? 1 : 3, 18);
}

constexpr int K3_STAGE = 48 * 1024;
constexpr int K3_NST = 3;
constexpr int K3_LDS = K3_NST * K3_STAGE + 2048;

__global__ __launch_bounds__(768) void k3_ffn1(const u16* __restrict__ h, const float2* __restrict__ stats,
                                               const u16* __restrict__ wg, const u16* __restrict__ wu,
                                               const float* __restrict__ colsum, u16* __restrict__ act) {
  extern __shared__ __attribute__((aligned(16))) char smem[];
  float2* sRS = (float2*)(smem + K3_NST * K3_STAGE);
  float* sCSm = (float*)(smem + K3_NST * K3_STAGE + 1024);
  const int tid = threadIdx.x, l = tid & 63;
  const int w = __builtin_amdgcn_readfirstlane(tid >> 6);
  const int bid = blockIdx.x;
  const int xcd = bid & 7, loc = bid >> 3;
  const int mblk = (xcd >> 1) * 4 + (loc & 3), fblk = (xcd & 1) * 7 + (loc >> 2);
  const int m0 = mblk * 128, f0 = fblk * 128;
  const int r = l >> 3, c = l & 7;
  const int row16 = l & 15, g = l >> 4, q = (l >> 2) & 3, p4 = l & 3;
  const int mp = w & 3, fh = w >> 2;
  STAMP(2, 0);
  if (w >= 8) {
    const int lw = w - 8;
    const int tp = c >> 1, half = c & 1;
    const unsigned s_off = lds_off(smem);
    const unsigned avo = (unsigned)((m0 + lw * 8 + r) * D_ + ((c ^ ((lw & 1) * 4 + (r >> 1))) * 8)) * 2u;
    const unsigned bvo = (unsigned)((lw * 8 + r) * F_ + f0 + 16 * (tp ^ (((r >> 1) & 1) | ((lw & 1) << 1))) + 8 * half) * 2u;
    auto issue = [&](int ch) {
      unsigned dst = s_off + (ch % K3_NST) * K3_STAGE;
#pragma unroll
      for (int i = 0; i < 4; ++i) glds16(h + ch * 64 + i * 32 * D_, avo, dst + (4 * i + lw) * 1024);
#pragma unroll
      for (int mc = 0; mc < 4; ++mc)
#pragma unroll
        for (int hk = 0; hk < 2; ++hk) {
          const u16* W = ((mc >> 1) ? wu : wg) + (size_t)(ch * 64 + hk * 32) * F_ + (mc & 1) * 64;
          glds16(W, bvo, dst + (16 + mc * 8 + hk * 4 + lw) * 1024);
        }
    };
#pragma unroll
    for (int ch = 0; ch < K3_NST - 1; ++ch) issue(ch);
#pragma unroll
    for (int ch = 0; ch < 7; ++ch) {
      WAIT_VMN(12 * cmin(K3_NST - 2, 6 - ch));
      BAR();
      if (ch + K3_NST - 1 < 7) issue(ch + K3_NST - 1);
    }
    return;
  }
  if (tid >= 128 && tid < 384) {
    int i = tid - 128;
    sCSm[i] = colsum[(i >> 7) * F_ + f0 + (i & 127)];
  }
  if (tid < 128) {
    const float2* sp = stats + (size_t)(m0 + tid) * 14;
    float2 p[14];
#pragma unroll
    for (int i = 0; i < 14; ++i) p[i] = sp[i];
    float tot = 0.f;
#pragma unroll
    for (int i = 0; i < 14; ++i) tot += p[i].x;
    float mean = tot * (1.0f / D_), m2 = 0.f;
#pragma unroll
    for (int i = 0; i < 14; ++i) { float dm = p[i].x * (1.0f / 32.0f) - mean; m2 += p[i].y + 32.0f * dm * dm; }
    sRS[tid] = make_float2(mean, 1.0f / sqrtf(m2 * (1.0f / D_) + 1e-5f));
  }

  const int aoff = (4 * mp + (row16 >> 3)) * 1024 + (row16 & 7) * 128;
  const int aswz = (row16 >> 1) & 7;
  const int boff0 = 16384 + fh * 8192 + g * 1024 + q * 128 + ((((q >> 1) & 1) | ((g & 1) << 1)) * 32) + p4 * 8;
  f32x4 acc[2][2][4];
#pragma unroll
  for (int m = 0; m < 2; ++m)
#pragma unroll
    for (int i = 0; i < 2; ++i)
#pragma unroll
      for (int j = 0; j < 4; ++j) acc[m][i][j] = (f32x4){0.f, 0.f, 0.f, 0.f};

#pragma unroll 1
  for (int ch = 0; ch < 7; ++ch) {
    STAMP(2, 2 + 2 * ch);
    WAIT_LGKM0();
    BAR();
    STAMP(2, 3 + 2 * ch);
    const char* stg = smem + (ch % K3_NST) * K3_STAGE;
#pragma unroll
    for (int ks2 = 0; ks2 < 2; ++ks2) {
      int cs = ((4 * ks2 + g) ^ aswz) * 16;
      u32x4 a0 = ldA(stg + aoff + cs), a1 = ldA(stg + aoff + 2048 + cs);
#pragma unroll
      for (int gu = 0; gu < 2; ++gu)
#pragma unroll
        for (int t = 0; t < 4; ++t) {
          bf16x8 b = ldB(stg + gu * 16384 + ks2 * 4096 + (boff0 ^ (t << 5)));
          acc[0][gu][t] = mfma(b, a0, acc[0][gu][t]);
          acc[1][gu][t] = mfma(b, a1, acc[1][gu][t]);
        }
    }
  }
  STAMP(2, 16);
#pragma unroll
  for (int m = 0; m < 2; ++m) {
    float2 rs = sRS[32 * mp + 16 * m + row16];
#pragma unroll
    for (int t = 0; t < 4; ++t) {
      float a4[4];
      f32x4 cg = *(const f32x4*)(sCSm + fh * 64 + 16 * t + 4 * g);
      f32x4 cu = *(const f32x4*)(sCSm + 128 + fh * 64 + 16 * t + 4 * g);
#pragma unroll
      for (int j = 0; j < 4; ++j) {
        float gv = (acc[m][0][t][j] - rs.x * cg[j]) * rs.y;
        float uv = (acc[m][1][t][j] - rs.x * cu[j]) * rs.y;
        gv = rbf(gv); uv = rbf(uv);
        float sg = rbf(gv * __builtin_amdgcn_rcpf(1.0f + __expf(-gv)));
        a4[j] = sg * uv;
      }
      u32x2 o2; o2[0] = pack_bf16(a4[0], a4[1]); o2[1] = pack_bf16(a4[2], a4[3]);
      *(u32x2*)(act + (size_t)(m0 + 32 * mp + 16 * m + row16) * F_ + f0 + fh * 64 + 16 * t + 4 * g) = o2;
    }
  }
  STAMP(2, 17);
}

constexpr int K2_NST = 8, K4_NST = 5;
#define K2_KERN gemm64<7, K2_NST, 1, true>
#define K4_KERN gemm64<14, K4_NST, 2, false>
constexpr int K2_LDS = K2_NST * 16384, K4_LDS = K4_NST * 32768;

extern "C" void kernel_launch(void* const* d_in, const int* in_sizes, int n_in, void* d_out, int out_size,
                              void* d_ws, size_t ws_size, hipStream_t stream) {
  const u16* x = (const u16*)d_in[0];
  const u16* wqkv = (const u16*)d_in[1];
  const u16* wo = (const u16*)d_in[2];
  const u16* wgate = (const u16*)d_in[4];
  const u16* wup = (const u16*)d_in[5];
  const u16* wdown = (const u16*)d_in[6];
  u16* out = (u16*)d_out;
  char* ws = (char*)d_ws;
  u16* o = (u16*)ws;
  u16* h = (u16*)(ws + (2u << 20));
  float2* stats = (float2*)(ws + (4u << 20));
  float* colsum = (float*)(ws + (6u << 20));
  u16* act = (u16*)(ws + (8u << 20));

  hipFuncSetAttribute((const void*)k1_qkv_attn, hipFuncAttributeMaxDynamicSharedMemorySize, K1_LDS);
  hipFuncSetAttribute((const void*)K2_KERN, hipFuncAttributeMaxDynamicSharedMemorySize, K2_LDS);
  hipFuncSetAttribute((const void*)K4_KERN, hipFuncAttributeMaxDynamicSharedMemorySize, K4_LDS);
  hipFuncSetAttribute((const void*)k3_ffn1, hipFuncAttributeMaxDynamicSharedMemorySize, K3_LDS);

  k1_qkv_attn<<<256, 768, K1_LDS, stream>>>(x, wqkv, o, wo, wgate, wup, wdown);
  K2_KERN<<<256, 768, K2_LDS, stream>>>(o, wo, x, h, stats, wgate, wup, colsum);
  k3_ffn1<<<224, 768, K3_LDS, stream>>>(h, stats, wgate, wup, colsum, act);
  K4_KERN<<<224, 768, K4_LDS, stream>>>(act, wdown, h, out, nullptr, nullptr, nullptr, nullptr);
}
```

```cpp
#include <hip/hip_runtime.h>
#include <hip/hip_bf16.h>
#include <stdint.h>

typedef __attribute__((ext_vector_type(8))) __bf16 bf16x8;
typedef __attribute__((ext_vector_type(2))) __bf16 bf16x2;
typedef __attribute__((ext_vector_type(4))) short s16x4;
typedef __attribute__((ext_vector_type(8))) short s16x8;
typedef __attribute__((ext_vector_type(4))) float f32x4;
typedef __attribute__((ext_vector_type(4))) unsigned u32x4;
typedef __attribute__((ext_vector_type(2))) unsigned u32x2;
typedef unsigned short u16;

#define LDS_AS __attribute__((address_space(3)))

#define NKS 2
constexpr int S_ = 2048, D_ = 448, F_ = 1792, QKVN = 1344;

__device__ __forceinline__ float bf_lo(unsigned u) { return __uint_as_float(u << 16); }
__device__ __forceinline__ float bf_hi(unsigned u) { return __uint_as_float(u & 0xffff0000u); }
__device__ __forceinline__ unsigned pack_bf16(float a, float b) {
  bf16x2 v; v[0] = (__bf16)a; v[1] = (__bf16)b;
  return __builtin_bit_cast(unsigned, v);
}
__device__ __forceinline__ float rbf(float a) {
  __bf16 b = (__bf16)a; return (float)b;
}
__device__ __forceinline__ float dot2bf(unsigned a, unsigned b, float c) {
  return __builtin_amdgcn_fdot2_f32_bf16(__builtin_bit_cast(bf16x2, a), __builtin_bit_cast(bf16x2, b), c, false);
}
__device__ __forceinline__ void glds16(const void* sbase, unsigned voff, unsigned lds_addr) {
  unsigned keep;
  asm volatile("s_mov_b32 %0, m0\n\ts_mov_b32 m0, %3\n\ts_nop 0\n\tglobal_load_lds_dwordx4 %1, %2\n\ts_mov_b32 m0, %0"
               : "=&s"(keep) : "v"(voff), "s"(sbase), "s"(lds_addr) : "memory");
}
template <int IMM>
__device__ __forceinline__ u32x4 gload16(const void* sbase, unsigned voff) {
  u32x4 r;
  asm volatile("global_load_dwordx4 %0, %1, %2 offset:%3" : "=v"(r) : "v"(voff), "s"(sbase), "n"(IMM) : "memory");
  return r;
}
template <int IMM>
__device__ __forceinline__ u32x2 gload8(const void* sbase, unsigned voff) {
  u32x2 r;
  asm volatile("global_load_dwordx2 %0, %1, %2 offset:%3" : "=v"(r) : "v"(voff), "s"(sbase), "n"(IMM) : "memory");
  return r;
}
__device__ __forceinline__ void gstore16_wt(void* p, u32x4 v) {
  asm volatile("global_store_dwordx4 %0, %1, off sc1\n\ts_nop 1" :: "v"(p), "v"(v) : "memory");
}
__device__ __forceinline__ unsigned lds_off(const void* p) { return (unsigned)(uintptr_t)(LDS_AS const char*)p; }
#define PIN(x) asm volatile("" : "+v"(x))
#define STAMP(kid, i)
#define WAIT_VMN(n) asm volatile("s_waitcnt vmcnt(%0)" :: "n"(n) : "memory")
#define WAIT_LGKM0() do { __builtin_amdgcn_s_waitcnt(0xC07F); asm volatile("" ::: "memory"); } while (0)
#define BAR() __builtin_amdgcn_s_barrier()
#define ILV(n) do { _Pragma("unroll") for (int z_ = 0; z_ < (n); ++z_) { __builtin_amdgcn_sched_group_barrier(0x8, 1, 0); \
  __builtin_amdgcn_sched_group_barrier(0x100, 1, 0); } } while (0)

__device__ __forceinline__ bf16x8 ldB(const char* p) {
  s16x4 a = __builtin_amdgcn_ds_read_tr16_b64_v4i16((LDS_AS s16x4*)(p));
  s16x4 b = __builtin_amdgcn_ds_read_tr16_b64_v4i16((LDS_AS s16x4*)(p + 512));
  s16x8 r = __builtin_shufflevector(a, b, 0, 1, 2, 3, 4, 5, 6, 7);
  return __builtin_bit_cast(bf16x8, r);
}
__device__ __forceinline__ u32x4 ldA(const char* p) { return *(const u32x4*)p; }
__device__ __forceinline__ f32x4 mfma(bf16x8 a, u32x4 b, f32x4 c) {
  return __builtin_amdgcn_mfma_f32_16x16x32_bf16(a, __builtin_bit_cast(bf16x8, b), c, 0, 0, 0);
}
constexpr int cmin(int a, int b) { return a < b ? a : b; }

constexpr int K1_STAGE = 34 * 1024;
constexpr int K1_NST = 4;
constexpr int K1_LDS = K1_NST * K1_STAGE + 2048;
constexpr int K1_QP = 392;

__global__ __launch_bounds__(768) void k1_qkv_attn(const u16* __restrict__ x, const u16* __restrict__ wqkv,
                                                   u16* __restrict__ o, const u16* __restrict__ wo,
                                                   const u16* __restrict__ wg, const u16* __restrict__ wu,
                                                   const u16* __restrict__ wd) {
  extern __shared__ __attribute__((aligned(16))) char smem[];
  float* sCS = (float*)(smem + K1_NST * K1_STAGE);
  const int tid = threadIdx.x, l = tid & 63;
  const int w = __builtin_amdgcn_readfirstlane(tid >> 6);
  const int bid = blockIdx.x;
  const int mblk = (bid & 7) * 4 + ((bid >> 3) & 3), hp = bid >> 5;
  const int m0 = mblk * 64;
  const int r = l >> 3, c = l & 7;
  STAMP(0, 0);
  if (w >= 8) {
    const int lw = w - 8;
    const unsigned s_off = lds_off(smem);
    const int asw = (c ^ ((lw & 1) * 4 + (r >> 1))) * 8;
    int grow = m0 - 16 + lw * 8 + r; grow = grow < 0 ? 0 : grow;
    const unsigned avo0 = (unsigned)(grow * D_ + asw) * 2u;
    const unsigned avo1 = (unsigned)((m0 + 16 + lw * 8 + r) * D_ + asw) * 2u;
    const unsigned avo2 = (unsigned)((m0 + 48 + lw * 8 + r) * D_ + asw) * 2u;
    const int tp = c >> 1, half = c & 1;
    const unsigned bvoff = (unsigned)((lw * 8 + r) * QKVN + hp * 56 + 16 * (tp ^ (((r >> 1) & 1) | ((lw & 1) << 1))) + 8 * half) * 2u;
    auto issue = [&](int ch) {
      unsigned dst = s_off + (ch % K1_NST) * K1_STAGE;
      glds16(x + ch * 64, avo0, dst + lw * 1024);
      glds16(x + ch * 64, avo1, dst + (4 + lw) * 1024);
      if (lw < 2) glds16(x + ch * 64, avo2, dst + (8 + lw) * 1024);
#pragma unroll
      for (int part = 0; part < 3; ++part)
#pragma unroll
        for (int hk = 0; hk < 2; ++hk) {
          unsigned vo = bvoff + (unsigned)((ch * 64 + hk * 32) * QKVN + part * D_) * 2u;
          vo = vo > (unsigned)(D_ * QKVN - 8) * 2u ? (unsigned)(D_ * QKVN - 8) * 2u : vo;
          glds16(wqkv, vo, dst + (10 + part * 8 + hk * 4 + lw) * 1024);
        }
    };
#pragma unroll
    for (int ch = 0; ch < K1_NST; ++ch) issue(ch);
#pragma unroll
    for (int b = 0; b < 7; ++b) {
      const int ahead = cmin(b == 0 ? K1_NST - 1 : K1_NST - 2, 6 - b);
      if (lw < 2) { if (ahead == 3) WAIT_VMN(27); else if (ahead == 2) WAIT_VMN(18); else if (ahead == 1) WAIT_VMN(9); else WAIT_VMN(0); }
      else { if (ahead == 3) WAIT_VMN(24); else if (ahead == 2) WAIT_VMN(16); else if (ahead == 1) WAIT_VMN(8); else WAIT_VMN(0); }
      BAR();
      if (b >= 1 && b - 1 + K1_NST < 7) issue(b - 1 + K1_NST);
    }
    return;
  }

  u32x4 pf[3];
#pragma unroll
  for (int i = 0; i < 3; ++i) {
    int u = bid * 20 + w + 8 * i;
    if (w + 8 * i >= 20 || u >= 5096) u = 0;
    const u16* base = u < 392 ? wo : (u < 1960 ? wg : (u < 3528 ? wu : wd));
    int ub = u < 392 ? u : (u < 1960 ? u - 392 : (u < 3528 ? u - 1960 : u - 3528));
    pf[i] = gload16<0>(base, (unsigned)(ub * 1024 + l * 16));
  }
  const int mp = w >> 2, nq = w & 3;
  const int row16 = l & 15, g = l >> 4, q = (l >> 2) & 3, p4 = l & 3;
  const int arow = (row16 >> 3) * 1024 + (row16 & 7) * 128;
  const int aswz = (row16 >> 1) & 7;
  const int boffb = 10240 + g * 1024 + q * 128 + ((((q >> 1) & 1) | ((g & 1) << 1)) * 32) + p4 * 8;
  int boff[3];
#pragma unroll
  for (int i = 0; i < 3; ++i) { int nt12 = 3 * nq + i; boff[i] = (nt12 >> 2) * 8192 + (boffb ^ ((nt12 & 3) << 5)); }
  f32x4 acc[3][3], gram[3];
#pragma unroll
  for (int i = 0; i < 3; ++i) {
    gram[i] = (f32x4){0.f, 0.f, 0.f, 0.f};
#pragma unroll
    for (int j = 0; j < 3; ++j) acc[i][j] = (f32x4){0.f, 0.f, 0.f, 0.f};
  }
  const unsigned ONES = 0x3f803f80u;
  struct FS { u32x4 a[3]; bf16x8 b[3]; };
  auto rd = [&](FS& f, int ch, int ks2) {
    const char* stg = smem + (ch % K1_NST) * K1_STAGE;
    int cs = ((4 * ks2 + g) ^ aswz) * 16;
    f.a[0] = ldA(stg + (4 * mp + 2) * 1024 + arow + cs);
    f.a[1] = ldA(stg + (4 * mp + 4) * 1024 + arow + cs);
#pragma unroll
    for (int i = 0; i < 3; ++i) f.b[i] = ldB(stg + ks2 * 4096 + boff[i]);
    if (mp == 0) f.a[2] = ldA(stg + arow + cs);
  };
  auto mm = [&](const FS& f) {
    bf16x8 b0 = f.b[0];
    if (nq == 1) {
      u32x4 u = __builtin_bit_cast(u32x4, b0);
#pragma unroll
      for (int j = 0; j < 4; ++j) u[j] = row16 == 15 ? ONES : u[j];
      b0 = __builtin_bit_cast(bf16x8, u);
    }
#pragma unroll
    for (int m = 0; m < 2; ++m) {
      acc[m][0] = mfma(b0, f.a[m], acc[m][0]);
      acc[m][1] = mfma(f.b[1], f.a[m], acc[m][1]);
      acc[m][2] = mfma(f.b[2], f.a[m], acc[m][2]);
      if (nq == 0) gram[m] = mfma(__builtin_bit_cast(bf16x8, f.a[m]), f.a[m], gram[m]);
    }
    if (mp == 0) {
      if (nq == 0) gram[2] = mfma(__builtin_bit_cast(bf16x8, f.a[2]), f.a[2], gram[2]);
      u32x4 ah;
#pragma unroll
      for (int j = 0; j < 4; ++j) ah[j] = row16 == 0 ? ONES : f.a[2][j];
      acc[2][0] = mfma(b0, ah, acc[2][0]);
      acc[2][1] = mfma(f.b[1], ah, acc[2][1]);
      acc[2][2] = mfma(f.b[2], ah, acc[2][2]);
    }
  };
  FS f0, f1;
  BAR();
  rd(f0, 0, 0);
#pragma unroll 1
  for (int ch = 0; ch < 6; ++ch) {
    WAIT_LGKM0();
    rd(f1, ch, 1);
    mm(f0);
    __builtin_amdgcn_sched_barrier(0);
    WAIT_LGKM0();
    BAR();
    rd(f0, ch + 1, 0);
    mm(f1);
    __builtin_amdgcn_sched_barrier(0);
  }
  WAIT_LGKM0();
  rd(f1, 6, 1);
  mm(f0);
  __builtin_amdgcn_sched_barrier(0);
  mm(f1);
  STAMP(0, 16);
  float* sST = sCS + 192;
  {
    const int trow[3] = {16 * (2 * mp + 1), 16 * (2 * mp + 2), 0};
#pragma unroll
    for (int m = 0; m < 3; ++m) {
      if (m == 2 && mp != 0) break;
      if (nq == 1 && g == 3) sST[trow[m] + row16] = acc[m][0][3];
      if (nq == 0 && g == (row16 >> 2)) {
        f32x4 gm = gram[m];
        sST[80 + trow[m] + row16] = (l & 3) == 0 ? gm[0] : ((l & 3) == 1 ? gm[1] : ((l & 3) == 2 ? gm[2] : gm[3]));
      }
    }
    if (mp == 0 && row16 == 0) {
#pragma unroll
      for (int i = 0; i < 3; ++i) *(f32x4*)(sCS + (3 * nq + i) * 16 + 4 * g) = acc[2][i];
    }
  }
  WAIT_LGKM0();
  BAR();

  char* sQ = smem;
  {
    f32x4 cs[3];
#pragma unroll
    for (int i = 0; i < 3; ++i) cs[i] = *(const f32x4*)(sCS + (3 * nq + i) * 16 + 4 * g);
#pragma unroll
    for (int m = 0; m < 3; ++m) {
      if (m == 2 && mp != 0) break;
      const int row = (m == 2 ? 0 : 16 * (2 * mp + 1 + m)) + row16;
      const float mean = sST[row] * (1.0f / D_);
      const float rstd = 1.0f / sqrtf(fmaxf(sST[80 + row] * (1.0f / D_) - mean * mean, 0.f) + 1e-5f);
#pragma unroll
      for (int i = 0; i < 3; ++i) {
        u32x2 v2;
        v2[0] = pack_bf16((acc[m][i][0] - mean * cs[i][0]) * rstd, (acc[m][i][1] - mean * cs[i][1]) * rstd);
        v2[1] = pack_bf16((acc[m][i][2] - mean * cs[i][2]) * rstd, (acc[m][i][3] - mean * cs[i][3]) * rstd);
        *(u32x2*)(sQ + row * K1_QP + ((3 * nq + i) * 16 + 4 * g) * 2) = v2;
      }
    }
  }
  WAIT_LGKM0();
  BAR();

  const int mt = w & 3, nh = w >> 2;
  {
    const int hd = nh;
    const int t0row = 16 * mt;
    u32x4 qf;
    {
      const char* qp = sQ + (t0row + 16 + row16) * K1_QP + hd * 56 + g * 16;
      u32x2 a0 = *(const u32x2*)qp, a1 = *(const u32x2*)(qp + 8);
      qf[0] = a0[0]; qf[1] = a0[1]; qf[2] = g == 3 ? 0u : a1[0]; qf[3] = g == 3 ? 0u : a1[1];
    }
    f32x4 sc[2];
#pragma unroll
    for (int kt = 0; kt < 2; ++kt) {
      const char* kp = sQ + (t0row + 16 * kt + row16) * K1_QP + 128 + hd * 56 + g * 16;
      u32x2 a0 = *(const u32x2*)kp, a1 = *(const u32x2*)(kp + 8);
      u32x4 kf; kf[0] = a0[0]; kf[1] = a0[1]; kf[2] = g == 3 ? 0u : a1[0]; kf[3] = g == 3 ? 0u : a1[1];
      sc[kt] = mfma(__builtin_bit_cast(bf16x8, kf), qf, (f32x4){0.f, 0.f, 0.f, 0.f});
    }
    float lg[8];
    float mx = -3.0e38f;
#pragma unroll
    for (int kt = 0; kt < 2; ++kt)
#pragma unroll
      for (int rr = 0; rr < 4; ++rr) {
        int dist = 16 + row16 - (16 * kt + 4 * g + rr);
        bool inwin = dist >= 0 && dist <= 14 && !(dist & 1);
        float d = rbf(rbf(sc[kt][rr]) * 0.18898223650461362f);
        d = (m0 + 16 * mt + row16 - dist >= 0) ? d : -1e9f;
        lg[kt * 4 + rr] = inwin ? d : -3.0e38f;
        mx = fmaxf(mx, lg[kt * 4 + rr]);
      }
    mx = fmaxf(mx, __shfl_xor(mx, 16)); mx = fmaxf(mx, __shfl_xor(mx, 32));
    float e[8], sm = 0.f;
#pragma unroll
    for (int i = 0; i < 8; ++i) { e[i] = lg[i] > -2.0e38f ? __expf(lg[i] - mx) : 0.f; sm += e[i]; }
    sm += __shfl_xor(sm, 16); sm += __shfl_xor(sm, 32);
    const float inv = 1.0f / sm;
    u32x4 pf4;
#pragma unroll
    for (int i = 0; i < 4; ++i) pf4[i] = pack_bf16(e[2 * i] * inv, e[2 * i + 1] * inv);
#pragma unroll
    for (int dt = 0; dt < 2; ++dt) {
      const char* vp = sQ + (t0row + 4 * g + q) * K1_QP + 256 + hd * 56 + dt * 32 + p4 * 8;
      s16x4 va = __builtin_amdgcn_ds_read_tr16_b64_v4i16((LDS_AS s16x4*)(vp));
      s16x4 vb = __builtin_amdgcn_ds_read_tr16_b64_v4i16((LDS_AS s16x4*)(vp + 16 * K1_QP));
      s16x8 vv = __builtin_shufflevector(va, vb, 0, 1, 2, 3, 4, 5, 6, 7);
      f32x4 oo = mfma(__builtin_bit_cast(bf16x8, vv), pf4, (f32x4){0.f, 0.f, 0.f, 0.f});
      if (dt == 0 || g < 3) {
        u32x2 s0; s0[0] = pack_bf16(oo[0], oo[1]); s0[1] = pack_bf16(oo[2], oo[3]);
        *(u32x2*)(o + (size_t)(m0 + 16 * mt + row16) * D_ + hp * 56 + hd * 28 + dt * 16 + 4 * g) = s0;
      }
    }
  }
  WAIT_VMN(0);
  PIN(pf[0]); PIN(pf[1]); PIN(pf[2]);
  STAMP(0, 17);
}

template <int NCH, int NST, int SUB, bool STATS>
__global__ __launch_bounds__(768) void gemm64(const u16* __restrict__ A, const u16* __restrict__ B,
                                              const u16* __restrict__ resid, u16* __restrict__ out,
                                              float2* __restrict__ stats, const u16* __restrict__ wg,
                                              const u16* __restrict__ wu, float* __restrict__ colsum) {
  extern __shared__ __attribute__((aligned(16))) char smem[];
  constexpr int LDA = NCH * SUB * 64;
  constexpr int STG = SUB * 16384;
  const int tid = threadIdx.x, l = tid & 63;
  const int w = __builtin_amdgcn_readfirstlane(tid >> 6);
  const int bid = blockIdx.x;
  if (STATS && bid >= 224) {
    const int j = bid - 224;
    if (j < 28 && tid < 512) {
      const u16* W = (j < 14 ? wg : wu) + (j % 14) * 128;
      const int cc = tid & 15, rs = tid >> 4;
      float a[8];
#pragma unroll
      for (int i = 0; i < 8; ++i) a[i] = 0.f;
#pragma unroll
      for (int i = 0; i < 14; ++i) {
        u32x4 v = *(const u32x4*)(W + (size_t)(rs + 32 * i) * F_ + cc * 8);
#pragma unroll
        for (int e = 0; e < 4; ++e) { a[2 * e] += bf_lo(v[e]); a[2 * e + 1] += bf_hi(v[e]); }
      }
      float* red = (float*)smem;
#pragma unroll
      for (int i = 0; i < 8; ++i) red[rs * 128 + cc * 8 + i] = a[i];
      __syncthreads();
      if (tid < 128) {
        float s = 0.f;
#pragma unroll
        for (int i = 0; i < 32; ++i) s += red[i * 128 + tid];
        colsum[j * 128 + tid] = s;
      }
    }
    return;
  }
  const int loc = bid >> 3;
  const int mblk = (bid & 7) * 4 + (loc & 3), nblk = loc >> 2;
  const int m0 = mblk * 64, n0 = nblk * 64;
  const int r = l >> 3, c = l & 7;
  const int row16 = l & 15, g = l >> 4, q = (l >> 2) & 3, p4 = l & 3;
  const int mt = w & 3, nh = w >> 2;
  STAMP(STATS ? 1 : 3, 0);

  if (w >= 8) {
    const int lw = w - 8;
    const int tp = c >> 1, half = c & 1;
    const unsigned s_off = lds_off(smem);
    const unsigned avo = (unsigned)((m0 + lw * 8 + r) * LDA + ((c ^ ((lw & 1) * 4 + (r >> 1))) * 8)) * 2u;
    const unsigned bvo = (unsigned)((lw * 8 + r) * D_ + n0 + 16 * (tp ^ (((r >> 1) & 1) | ((lw & 1) << 1))) + 8 * half) * 2u;
    auto issue = [&](int ch) {
#pragma unroll
      for (int sub = 0; sub < SUB; ++sub) {
        unsigned dst = s_off + (ch % NST) * STG + sub * 16384;
        const int k0 = (ch * SUB + sub) * 64;
        glds16(A + k0, avo, dst + lw * 1024);
        glds16(A + k0 + 32 * LDA, avo, dst + (4 + lw) * 1024);
        glds16(B + (size_t)k0 * D_, bvo, dst + 8192 + lw * 1024);
        glds16(B + (size_t)(k0 + 32) * D_, bvo, dst + 8192 + (4 + lw) * 1024);
      }
    };
#pragma unroll
    for (int ch = 0; ch < cmin(NST, NCH); ++ch) issue(ch);
#pragma unroll
    for (int b = 0; b < NCH; ++b) {
      WAIT_VMN(4 * SUB * cmin(b == 0 ? NST - 1 : NST - 2, NCH - 1 - b));
      BAR();
      if (b >= 1 && b - 1 + NST < NCH) issue(b - 1 + NST);
    }
    return;
  }
  u32x2 rv[2];
  {
    unsigned voff = (unsigned)((m0 + 16 * mt + row16) * D_ + n0 + nh * 32 + 4 * g) * 2u;
    rv[0] = gload8<0>(resid, voff); rv[1] = gload8<32>(resid, voff);
  }
  constexpr int KQ = 2 * SUB, MT = SUB == 2 ? 4 : 2;
  const int cn = w & 1, kq = (w >> 1) & (KQ - 1), cm = SUB == 2 ? 0 : (w >> 2);
  const int aoff = (kq >> 1) * 16384 + (cm * 4 + (row16 >> 3)) * 1024 + (row16 & 7) * 128 + (((4 * (kq & 1) + g) ^ ((row16 >> 1) & 7)) * 16);
  const int boff0 = (kq >> 1) * 16384 + 8192 + (kq & 1) * 4096 + g * 1024 + q * 128 + ((((q >> 1) & 1) | ((g & 1) << 1)) * 32) + p4 * 8;
  f32x4 pacc[MT][2];
#pragma unroll
  for (int m = 0; m < MT; ++m) { pacc[m][0] = (f32x4){0.f, 0.f, 0.f, 0.f}; pacc[m][1] = pacc[m][0]; }
  struct FS { u32x4 a[MT]; bf16x8 b[2]; };
  auto rd = [&](FS& f, int ch) {
    const char* st = smem + (ch % NST) * STG;
#pragma unroll
    for (int t = 0; t < 2; ++t) f.b[t] = ldB(st + (boff0 ^ ((2 * cn + t) << 5)));
#pragma unroll
    for (int m = 0; m < MT; ++m) f.a[m] = ldA(st + aoff + m * 2048);
  };
  auto mm = [&](const FS& f) {
#pragma unroll
    for (int m = 0; m < MT; ++m)
#pragma unroll
      for (int t = 0; t < 2; ++t) pacc[m][t] = mfma(f.b[t], f.a[m], pacc[m][t]);
  };
  FS f0, f1;
  BAR();
  rd(f0, 0);
#pragma unroll 1
  for (int ch = 0; ch + 2 < NCH; ch += 2) {
    WAIT_LGKM0(); BAR(); rd(f1, ch + 1);
    mm(f0);
    __builtin_amdgcn_sched_barrier(0);
    WAIT_LGKM0(); BAR(); rd(f0, ch + 2);
    mm(f1);
    __builtin_amdgcn_sched_barrier(0);
  }
  if (NCH % 2 == 0) {
    WAIT_LGKM0(); BAR(); rd(f1, NCH - 1);
    mm(f0);
    __builtin_amdgcn_sched_barrier(0);
    mm(f1);
  } else {
    mm(f0);
  }
  {
    f32x4* red = (f32x4*)smem;
#pragma unroll
    for (int m = 0; m < MT; ++m)
#pragma unroll
      for (int t = 0; t < 2; ++t) red[((kq * 4 + cm * 2 + m) * 4 + 2 * cn + t) * 64 + l] = pacc[m][t];
  }
  WAIT_LGKM0();
  BAR();
  f32x4 acc[2];
  {
    const f32x4* red = (const f32x4*)smem;
#pragma unroll
    for (int t = 0; t < 2; ++t) {
      acc[t] = red[((0 * 4 + mt) * 4 + 2 * nh + t) * 64 + l];
#pragma unroll
      for (int k = 1; k < KQ; ++k) acc[t] += red[((k * 4 + mt) * 4 + 2 * nh + t) * 64 + l];
    }
  }
  WAIT_VMN(0);
  PIN(rv[0]); PIN(rv[1]);
  float hv[2][4];
#pragma unroll
  for (int t = 0; t < 2; ++t) {
    hv[t][0] = rbf(bf_lo(rv[t][0]) + rbf(acc[t][0])); hv[t][1] = rbf(bf_hi(rv[t][0]) + rbf(acc[t][1]));
    hv[t][2] = rbf(bf_lo(rv[t][1]) + rbf(acc[t][2])); hv[t][3] = rbf(bf_hi(rv[t][1]) + rbf(acc[t][3]));
    u32x2 o2; o2[0] = pack_bf16(hv[t][0], hv[t][1]); o2[1] = pack_bf16(hv[t][2], hv[t][3]);
    *(u32x2*)(smem + 65536 + (16 * mt + row16) * 144 + (nh * 32 + 16 * t + 4 * g) * 2) = o2;
  }
  WAIT_LGKM0();
  BAR();
  {
    u32x4 v = *(const u32x4*)(smem + 65536 + (w * 8 + r) * 144 + c * 16);
    gstore16_wt(out + (size_t)(m0 + w * 8 + r) * D_ + n0 + c * 8, v);
  }
  if (STATS) {
    float s = 0.f;
#pragma unroll
    for (int t = 0; t < 2; ++t) s += hv[t][0] + hv[t][1] + hv[t][2] + hv[t][3];
    s += __shfl_xor(s, 16); s += __shfl_xor(s, 32);
    float mean = s * (1.0f / 32.0f), m2 = 0.f;
#pragma unroll
    for (int t = 0; t < 2; ++t)
#pragma unroll
      for (int j = 0; j < 4; ++j) { float d = hv[t][j] - mean; m2 += d * d; }
    m2 += __shfl_xor(m2, 16); m2 += __shfl_xor(m2, 32);
    if (g == 0) stats[(size_t)(m0 + 16 * mt + row16) * 14 + nblk * 2 + nh] = make_float2(s, m2);
  }
  STAMP(STATS ? 1 : 3, 18);
}

constexpr int K3_STAGE = 48 * 1024;
constexpr int K3_NST = 3;
constexpr int K3_LDS = K3_NST * K3_STAGE + 2048;

__global__ __launch_bounds__(768) void k3_ffn1(const u16* __restrict__ h, const float2* __restrict__ stats,
                                               const u16* __restrict__ wg, const u16* __restrict__ wu,
                                               const float* __restrict__ colsum, u16* __restrict__ act) {
  extern __shared__ __attribute__((aligned(16))) char smem[];
  float2* sRS = (float2*)(smem + K3_NST * K3_STAGE);
  float* sCSm = (float*)(smem + K3_NST * K3_STAGE + 1024);
  const int tid = threadIdx.x, l = tid & 63;
  const int w = __builtin_amdgcn_readfirstlane(tid >> 6);
  const int bid = blockIdx.x;
  const int xcd = bid & 7, loc = bid >> 3;
  const int mblk = (xcd >> 1) * 4 + (loc & 3), fblk = (xcd & 1) * 7 + (loc >> 2);
  const int m0 = mblk * 128, f0 = fblk * 128;
  const int r = l >> 3, c = l & 7;
  const int row16 = l & 15, g = l >> 4, q = (l >> 2) & 3, p4 = l & 3;
  STAMP(2, 0);
  if (w >= 8) {
    const int lw = w - 8;
    const int tp = c >> 1, half = c & 1;
    const unsigned s_off = lds_off(smem);
    const unsigned avo = (unsigned)((m0 + lw * 8 + r) * D_ + ((c ^ ((lw & 1) * 4 + (r >> 1))) * 8)) * 2u;
    const unsigned bvo = (unsigned)((lw * 8 + r) * F_ + f0 + 16 * (tp ^ (((r >> 1) & 1) | ((lw & 1) << 1))) + 8 * half) * 2u;
    auto issue = [&](int ch) {
      unsigned dst = s_off + (ch % K3_NST) * K3_STAGE;
#pragma unroll
      for (int i = 0; i < 4; ++i) glds16(h + ch * 64 + i * 32 * D_, avo, dst + (4 * i + lw) * 1024);
#pragma unroll
      for (int mc = 0; mc < 4; ++mc)
#pragma unroll
        for (int hk = 0; hk < 2; ++hk) {
          const u16* W = ((mc >> 1) ? wu : wg) + (size_t)(ch * 64 + hk * 32) * F_ + (mc & 1) * 64;
          glds16(W, bvo, dst + (16 + mc * 8 + hk * 4 + lw) * 1024);
        }
    };
#pragma unroll
    for (int ch = 0; ch < K3_NST; ++ch) issue(ch);
#pragma unroll
    for (int b = 0; b < 7; ++b) {
      WAIT_VMN(12 * cmin(b == 0 ? K3_NST - 1 : K3_NST - 2, 6 - b));
      BAR();
      if (b >= 1 && b - 1 + K3_NST < 7) issue(b - 1 + K3_NST);
    }
    return;
  }
  if (tid >= 128 && tid < 384) {
    int i = tid - 128;
    sCSm[i] = colsum[(i >> 7) * F_ + f0 + (i & 127)];
  }
  if (tid < 128) {
    const float2* sp = stats + (size_t)(m0 + tid) * 14;
    float2 p[14];
#pragma unroll
    for (int i = 0; i < 14; ++i) p[i] = sp[i];
    float tot = 0.f;
#pragma unroll
    for (int i = 0; i < 14; ++i) tot += p[i].x;
    float mean = tot * (1.0f / D_), m2 = 0.f;
#pragma unroll
    for (int i = 0; i < 14; ++i) { float dm = p[i].x * (1.0f / 32.0f) - mean; m2 += p[i].y + 32.0f * dm * dm; }
    sRS[tid] = make_float2(mean, 1.0f / sqrtf(m2 * (1.0f / D_) + 1e-5f));
  }

  const int mh = w & 1, nq = w >> 1;
  const int aoff = (8 * mh + (row16 >> 3)) * 1024 + (row16 & 7) * 128;
  const int aswz = (row16 >> 1) & 7;
  const int boffb = 16384 + (nq >> 1) * 8192 + g * 1024 + q * 128 + ((((q >> 1) & 1) | ((g & 1) << 1)) * 32) + p4 * 8;
  const int boff0 = boffb ^ ((2 * (nq & 1)) << 5), boff1 = boffb ^ ((2 * (nq & 1) + 1) << 5);
  f32x4 acc[4][2][2];
#pragma unroll
  for (int m = 0; m < 4; ++m)
#pragma unroll
    for (int i = 0; i < 2; ++i)
#pragma unroll
      for (int j = 0; j < 2; ++j) acc[m][i][j] = (f32x4){0.f, 0.f, 0.f, 0.f};

  struct FS { u32x4 a[4]; bf16x8 b[2][2]; };
  auto rd = [&](FS& f, int ch, int ks2) {
    const char* stg = smem + (ch % K3_NST) * K3_STAGE;
    int cs = ((4 * ks2 + g) ^ aswz) * 16;
#pragma unroll
    for (int gu = 0; gu < 2; ++gu) {
      f.b[gu][0] = ldB(stg + gu * 16384 + ks2 * 4096 + boff0);
      f.b[gu][1] = ldB(stg + gu * 16384 + ks2 * 4096 + boff1);
    }
#pragma unroll
    for (int m = 0; m < 4; ++m) f.a[m] = ldA(stg + aoff + m * 2048 + cs);
  };
  auto mm = [&](const FS& f) {
#pragma unroll
    for (int m = 0; m < 4; ++m)
#pragma unroll
      for (int gu = 0; gu < 2; ++gu)
#pragma unroll
        for (int t = 0; t < 2; ++t) acc[m][gu][t] = mfma(f.b[gu][t], f.a[m], acc[m][gu][t]);
  };
  FS fa, fb;
  WAIT_LGKM0();
  BAR();
  rd(fa, 0, 0);
#pragma unroll 1
  for (int ch = 0; ch < 6; ++ch) {
    WAIT_LGKM0();
    rd(fb, ch, 1);
    mm(fa);
    ILV(12);
    __builtin_amdgcn_sched_barrier(0);
    WAIT_LGKM0();
    BAR();
    rd(fa, ch + 1, 0);
    mm(fb);
    ILV(12);
    __builtin_amdgcn_sched_barrier(0);
  }
  WAIT_LGKM0();
  rd(fb, 6, 1);
  mm(fa);
  ILV(12);
  __builtin_amdgcn_sched_barrier(0);
  f32x4 cg[2], cu[2];
  float2 rs[4];
#pragma unroll
  for (int t = 0; t < 2; ++t) {
    cg[t] = *(const f32x4*)(sCSm + nq * 32 + 16 * t + 4 * g);
    cu[t] = *(const f32x4*)(sCSm + 128 + nq * 32 + 16 * t + 4 * g);
  }
#pragma unroll
  for (int m = 0; m < 4; ++m) rs[m] = sRS[64 * mh + 16 * m + row16];
  mm(fb);
  STAMP(2, 16);
  {
    float gv[4][2][4], uv[4][2][4], ex[4][2][4];
#pragma unroll
    for (int m = 0; m < 4; ++m)
#pragma unroll
      for (int t = 0; t < 2; ++t)
#pragma unroll
        for (int j = 0; j < 4; ++j) {
          gv[m][t][j] = rbf((acc[m][0][t][j] - rs[m].x * cg[t][j]) * rs[m].y);
          uv[m][t][j] = rbf((acc[m][1][t][j] - rs[m].x * cu[t][j]) * rs[m].y);
        }
    __builtin_amdgcn_sched_barrier(0);
#pragma unroll
    for (int m = 0; m < 4; ++m)
#pragma unroll
      for (int t = 0; t < 2; ++t)
#pragma unroll
        for (int j = 0; j < 4; ++j) ex[m][t][j] = __expf(-gv[m][t][j]);
    __builtin_amdgcn_sched_barrier(0);
#pragma unroll
    for (int m = 0; m < 4; ++m)
#pragma unroll
      for (int t = 0; t < 2; ++t)
#pragma unroll
        for (int j = 0; j < 4; ++j) ex[m][t][j] = __builtin_amdgcn_rcpf(1.0f + ex[m][t][j]);
    __builtin_amdgcn_sched_barrier(0);
#pragma unroll
    for (int m = 0; m < 4; ++m)
#pragma unroll
      for (int t = 0; t < 2; ++t) {
        float a4[4];
#pragma unroll
        for (int j = 0; j < 4; ++j) a4[j] = rbf(gv[m][t][j] * ex[m][t][j]) * uv[m][t][j];
        u32x2 o2; o2[0] = pack_bf16(a4[0], a4[1]); o2[1] = pack_bf16(a4[2], a4[3]);
        *(u32x2*)(smem + K3_STAGE + (64 * mh + 16 * m + row16) * 272 + (nq * 32 + 16 * t + 4 * g) * 2) = o2;
      }
  }
  WAIT_LGKM0();
  BAR();
#pragma unroll
  for (int i = 0; i < 4; ++i) {
    int row = (w * 4 + i) * 4 + (l >> 4);
    u32x4 v = *(const u32x4*)(smem + K3_STAGE + row * 272 + (l & 15) * 16);
    gstore16_wt(act + (size_t)(m0 + row) * F_ + f0 + (l & 15) * 8, v);
  }
  STAMP(2, 17);
}

constexpr int K2_NST = 8, K4_NST = 5;
#define K2_KERN gemm64<7, K2_NST, 1, true>
#define K4_KERN gemm64<14, K4_NST, 2, false>
constexpr int K2_LDS = K2_NST * 16384, K4_LDS = K4_NST * 32768;

#define NKERN 4
static void launch_one(int k, void* const* d_in, void* d_out, void* d_ws, hipStream_t stream) {
  const u16* x = (const u16*)d_in[0];
  const u16* wqkv = (const u16*)d_in[1];
  const u16* wo = (const u16*)d_in[2];
  const u16* wgate = (const u16*)d_in[4];
  const u16* wup = (const u16*)d_in[5];
  const u16* wdown = (const u16*)d_in[6];
  u16* out = (u16*)d_out;
  char* ws = (char*)d_ws;
  u16* o = (u16*)ws;
  u16* h = (u16*)(ws + (2u << 20));
  float2* stats = (float2*)(ws + (4u << 20));
  float* colsum = (float*)(ws + (6u << 20));
  u16* act = (u16*)(ws + (8u << 20));
  if (k == 0) k1_qkv_attn<<<256, 768, K1_LDS, stream>>>(x, wqkv, o, wo, wgate, wup, wdown);
  if (k == 1) K2_KERN<<<256, 768, K2_LDS, stream>>>(o, wo, x, h, stats, wgate, wup, colsum);
  if (k == 2) k3_ffn1<<<224, 768, K3_LDS, stream>>>(h, stats, wgate, wup, colsum, act);
  if (k == 3) K4_KERN<<<224, 768, K4_LDS, stream>>>(act, wdown, h, out, nullptr, nullptr, nullptr, nullptr);
}

extern "C" void kernel_launch(void* const* d_in, const int* in_sizes, int n_in, void* d_out, int out_size,
                              void* d_ws, size_t ws_size, hipStream_t stream) {
  hipFuncSetAttribute((const void*)k1_qkv_attn, hipFuncAttributeMaxDynamicSharedMemorySize, K1_LDS);
  hipFuncSetAttribute((const void*)K2_KERN, hipFuncAttributeMaxDynamicSharedMemorySize, K2_LDS);
  hipFuncSetAttribute((const void*)K4_KERN, hipFuncAttributeMaxDynamicSharedMemorySize, K4_LDS);
  hipFuncSetAttribute((const void*)k3_ffn1, hipFuncAttributeMaxDynamicSharedMemorySize, K3_LDS);
  for (int k = 0; k < NKERN; ++k) launch_one(k, d_in, d_out, d_ws, stream);
}
```

```cpp
#include <hip/hip_runtime.h>
#include <hip/hip_bf16.h>
#include <stdint.h>

typedef __attribute__((ext_vector_type(8))) __bf16 bf16x8;
typedef __attribute__((ext_vector_type(2))) __bf16 bf16x2;
typedef __attribute__((ext_vector_type(4))) short s16x4;
typedef __attribute__((ext_vector_type(8))) short s16x8;
typedef __attribute__((ext_vector_type(4))) float f32x4;
typedef __attribute__((ext_vector_type(4))) unsigned u32x4;
typedef __attribute__((ext_vector_type(2))) unsigned u32x2;
typedef unsigned short u16;

#define LDS_AS __attribute__((address_space(3)))

constexpr int S_ = 2048, D_ = 448, F_ = 1792, QKVN = 1344;

__device__ __forceinline__ float bf_lo(unsigned u) { return __uint_as_float(u << 16); }
__device__ __forceinline__ float bf_hi(unsigned u) { return __uint_as_float(u & 0xffff0000u); }
__device__ __forceinline__ unsigned pack_bf16(float a, float b) {
  bf16x2 v; v[0] = (__bf16)a; v[1] = (__bf16)b;
  return __builtin_bit_cast(unsigned, v);
}
__device__ __forceinline__ float rbf(float a) {
  __bf16 b = (__bf16)a; return (float)b;
}
__device__ __forceinline__ float dot2bf(unsigned a, unsigned b, float c) {
  return __builtin_amdgcn_fdot2_f32_bf16(__builtin_bit_cast(bf16x2, a), __builtin_bit_cast(bf16x2, b), c, false);
}
__device__ __forceinline__ void glds16(const void* sbase, unsigned voff, unsigned lds_addr) {
  unsigned keep;
  asm volatile("s_mov_b32 %0, m0\n\ts_mov_b32 m0, %3\n\ts_nop 0\n\tglobal_load_lds_dwordx4 %1, %2\n\ts_mov_b32 m0, %0"
               : "=&s"(keep) : "v"(voff), "s"(sbase), "s"(lds_addr) : "memory");
}
template <int IMM>
__device__ __forceinline__ u32x4 gload16(const void* sbase, unsigned voff) {
  u32x4 r;
  asm volatile("global_load_dwordx4 %0, %1, %2 offset:%3" : "=v"(r) : "v"(voff), "s"(sbase), "n"(IMM) : "memory");
  return r;
}
template <int IMM>
__device__ __forceinline__ u32x2 gload8(const void* sbase, unsigned voff) {
  u32x2 r;
  asm volatile("global_load_dwordx2 %0, %1, %2 offset:%3" : "=v"(r) : "v"(voff), "s"(sbase), "n"(IMM) : "memory");
  return r;
}
__device__ __forceinline__ void gstore16_wt(void* p, u32x4 v) {
  asm volatile("global_store_dwordx4 %0, %1, off sc1\n\ts_nop 1" :: "v"(p), "v"(v) : "memory");
}
__device__ __forceinline__ unsigned lds_off(const void* p) { return (unsigned)(uintptr_t)(LDS_AS const char*)p; }
#define PIN(x) asm volatile("" : "+v"(x))
#define WAIT_VMN(n) asm volatile("s_waitcnt vmcnt(%0)" :: "n"(n) : "memory")
#define WAIT_LGKM0() do { __builtin_amdgcn_s_waitcnt(0xC07F); asm volatile("" ::: "memory"); } while (0)
#define BAR() __builtin_amdgcn_s_barrier()
#define ILV(n) do { _Pragma("unroll") for (int z_ = 0; z_ < (n); ++z_) { __builtin_amdgcn_sched_group_barrier(0x8, 1, 0); \
  __builtin_amdgcn_sched_group_barrier(0x100, 1, 0); } } while (0)

__device__ __forceinline__ bf16x8 ldB(const char* p) {
  s16x4 a = __builtin_amdgcn_ds_read_tr16_b64_v4i16((LDS_AS s16x4*)(p));
  s16x4 b = __builtin_amdgcn_ds_read_tr16_b64_v4i16((LDS_AS s16x4*)(p + 512));
  s16x8 r = __builtin_shufflevector(a, b, 0, 1, 2, 3, 4, 5, 6, 7);
  return __builtin_bit_cast(bf16x8, r);
}
__device__ __forceinline__ u32x4 ldA(const char* p) { return *(const u32x4*)p; }
__device__ __forceinline__ f32x4 mfma(bf16x8 a, u32x4 b, f32x4 c) {
  return __builtin_amdgcn_mfma_f32_16x16x32_bf16(a, __builtin_bit_cast(bf16x8, b), c, 0, 0, 0);
}
constexpr int cmin(int a, int b) { return a < b ? a : b; }

constexpr int K1_STAGE = 34 * 1024;
constexpr int K1_NST = 4;
constexpr int K1_LDS = K1_NST * K1_STAGE + 2048;
constexpr int K1_QP = 392;

__global__ __launch_bounds__(768) void k1_qkv_attn(const u16* __restrict__ x, const u16* __restrict__ wqkv,
                                                   u16* __restrict__ o, const u16* __restrict__ wo,
                                                   const u16* __restrict__ wg, const u16* __restrict__ wu,
                                                   const u16* __restrict__ wd) {
  extern __shared__ __attribute__((aligned(16))) char smem[];
  float* sCS = (float*)(smem + K1_NST * K1_STAGE);
  const int tid = threadIdx.x, l = tid & 63;
  const int w = __builtin_amdgcn_readfirstlane(tid >> 6);
  const int bid = blockIdx.x;
  const int mblk = (bid & 7) * 4 + ((bid >> 3) & 3), hp = bid >> 5;
  const int m0 = mblk * 64;
  const int r = l >> 3, c = l & 7;
  if (w >= 8) {
    const int lw = w - 8;
    const unsigned s_off = lds_off(smem);
    const int asw = (c ^ ((lw & 1) * 4 + (r >> 1))) * 8;
    int grow = m0 - 16 + lw * 8 + r; grow = grow < 0 ? 0 : grow;
    const unsigned avo0 = (unsigned)(grow * D_ + asw) * 2u;
    const unsigned avo1 = (unsigned)((m0 + 16 + lw * 8 + r) * D_ + asw) * 2u;
    const unsigned avo2 = (unsigned)((m0 + 48 + lw * 8 + r) * D_ + asw) * 2u;
    const int tp = c >> 1, half = c & 1;
    const unsigned bvoff = (unsigned)((lw * 8 + r) * QKVN + hp * 56 + 16 * (tp ^ (((r >> 1) & 1) | ((lw & 1) << 1))) + 8 * half) * 2u;
    auto issue = [&](int ch) {
      unsigned dst = s_off + (ch % K1_NST) * K1_STAGE;
      glds16(x + ch * 64, avo0, dst + lw * 1024);
      glds16(x + ch * 64, avo1, dst + (4 + lw) * 1024);
      if (lw < 2) glds16(x + ch * 64, avo2, dst + (8 + lw) * 1024);
#pragma unroll
      for (int part = 0; part < 3; ++part)
#pragma unroll
        for (int hk = 0; hk < 2; ++hk) {
          unsigned vo = bvoff + (unsigned)((ch * 64 + hk * 32) * QKVN + part * D_) * 2u;
          vo = vo > (unsigned)(D_ * QKVN - 8) * 2u ? (unsigned)(D_ * QKVN - 8) * 2u : vo;
          glds16(wqkv, vo, dst + (10 + part * 8 + hk * 4 + lw) * 1024);
        }
    };
#pragma unroll
    for (int ch = 0; ch < K1_NST; ++ch) issue(ch);
#pragma unroll
    for (int b = 0; b < 7; ++b) {
      const int ahead = cmin(b == 0 ? K1_NST - 1 : K1_NST - 2, 6 - b);
      if (lw < 2) { if (ahead == 3) WAIT_VMN(27); else if (ahead == 2) WAIT_VMN(18); else if (ahead == 1) WAIT_VMN(9); else WAIT_VMN(0); }
      else { if (ahead == 3) WAIT_VMN(24); else if (ahead == 2) WAIT_VMN(16); else if (ahead == 1) WAIT_VMN(8); else WAIT_VMN(0); }
      BAR();
      if (b >= 1 && b - 1 + K1_NST < 7) issue(b - 1 + K1_NST);
    }
    return;
  }

  u32x4 pf[3];
#pragma unroll
  for (int i = 0; i < 3; ++i) {
    int u = bid * 20 + w + 8 * i;
    if (w + 8 * i >= 20 || u >= 5096) u = 0;
    const u16* base = u < 392 ? wo : (u < 1960 ? wg : (u < 3528 ? wu : wd));
    int ub = u < 392 ? u : (u < 1960 ? u - 392 : (u < 3528 ? u - 1960 : u - 3528));
    pf[i] = gload16<0>(base, (unsigned)(ub * 1024 + l * 16));
  }
  const int mp = w >> 2, nq = w & 3;
  const int row16 = l & 15, g = l >> 4, q = (l >> 2) & 3, p4 = l & 3;
  const int arow = (row16 >> 3) * 1024 + (row16 & 7) * 128;
  const int aswz = (row16 >> 1) & 7;
  const int boffb = 10240 + g * 1024 + q * 128 + ((((q >> 1) & 1) | ((g & 1) << 1)) * 32) + p4 * 8;
  int boff[3];
#pragma unroll
  for (int i = 0; i < 3; ++i) { int nt12 = 3 * nq + i; boff[i] = (nt12 >> 2) * 8192 + (boffb ^ ((nt12 & 3) << 5)); }
  f32x4 acc[3][3], gram[3];
#pragma unroll
  for (int i = 0; i < 3; ++i) {
    gram[i] = (f32x4){0.f, 0.f, 0.f, 0.f};
#pragma unroll
    for (int j = 0; j < 3; ++j) acc[i][j] = (f32x4){0.f, 0.f, 0.f, 0.f};
  }
  const unsigned ONES = 0x3f803f80u;
  struct FS { u32x4 a[3]; bf16x8 b[3]; };
  auto rd = [&](FS& f, int ch, int ks2) {
    const char* stg = smem + (ch % K1_NST) * K1_STAGE;
    int cs = ((4 * ks2 + g) ^ aswz) * 16;
    f.a[0] = ldA(stg + (4 * mp + 2) * 1024 + arow + cs);
    f.a[1] = ldA(stg + (4 * mp + 4) * 1024 + arow + cs);
#pragma unroll
    for (int i = 0; i < 3; ++i) f.b[i] = ldB(stg + ks2 * 4096 + boff[i]);
    if (mp == 0) f.a[2] = ldA(stg + arow + cs);
  };
  auto mm = [&](const FS& f) {
    bf16x8 b0 = f.b[0];
    if (nq == 1) {
      u32x4 u = __builtin_bit_cast(u32x4, b0);
#pragma unroll
      for (int j = 0; j < 4; ++j) u[j] = row16 == 15 ? ONES : u[j];
      b0 = __builtin_bit_cast(bf16x8, u);
    }
#pragma unroll
    for (int m = 0; m < 2; ++m) {
      acc[m][0] = mfma(b0, f.a[m], acc[m][0]);
      acc[m][1] = mfma(f.b[1], f.a[m], acc[m][1]);
      acc[m][2] = mfma(f.b[2], f.a[m], acc[m][2]);
      if (nq == 0) gram[m] = mfma(__builtin_bit_cast(bf16x8, f.a[m]), f.a[m], gram[m]);
    }
    if (mp == 0) {
      if (nq == 0) gram[2] = mfma(__builtin_bit_cast(bf16x8, f.a[2]), f.a[2], gram[2]);
      u32x4 ah;
#pragma unroll
      for (int j = 0; j < 4; ++j) ah[j] = row16 == 0 ? ONES : f.a[2][j];
      acc[2][0] = mfma(b0, ah, acc[2][0]);
      acc[2][1] = mfma(f.b[1], ah, acc[2][1]);
      acc[2][2] = mfma(f.b[2], ah, acc[2][2]);
    }
  };
  FS f0, f1;
  BAR();
  rd(f0, 0, 0);
#pragma unroll 1
  for (int ch = 0; ch < 6; ++ch) {
    WAIT_LGKM0();
    rd(f1, ch, 1);
    mm(f0);
    __builtin_amdgcn_sched_barrier(0);
    WAIT_LGKM0();
    BAR();
    rd(f0, ch + 1, 0);
    mm(f1);
    __builtin_amdgcn_sched_barrier(0);
  }
  WAIT_LGKM0();
  rd(f1, 6, 1);
  mm(f0);
  __builtin_amdgcn_sched_barrier(0);
  mm(f1);
  float* sST = sCS + 192;
  {
    const int trow[3] = {16 * (2 * mp + 1), 16 * (2 * mp + 2), 0};
#pragma unroll
    for (int m = 0; m < 3; ++m) {
      if (m == 2 && mp != 0) break;
      if (nq == 1 && g == 3) sST[trow[m] + row16] = acc[m][0][3];
      if (nq == 0 && g == (row16 >> 2)) {
        f32x4 gm = gram[m];
        sST[80 + trow[m] + row16] = (l & 3) == 0 ? gm[0] : ((l & 3) == 1 ? gm[1] : ((l & 3) == 2 ? gm[2] : gm[3]));
      }
    }
    if (mp == 0 && row16 == 0) {
#pragma unroll
      for (int i = 0; i < 3; ++i) *(f32x4*)(sCS + (3 * nq + i) * 16 + 4 * g) = acc[2][i];
    }
  }
  WAIT_LGKM0();
  BAR();

  char* sQ = smem;
  {
    f32x4 cs[3];
#pragma unroll
    for (int i = 0; i < 3; ++i) cs[i] = *(const f32x4*)(sCS + (3 * nq + i) * 16 + 4 * g);
#pragma unroll
    for (int m = 0; m < 3; ++m) {
      if (m == 2 && mp != 0) break;
      const int row = (m == 2 ? 0 : 16 * (2 * mp + 1 + m)) + row16;
      const float mean = sST[row] * (1.0f / D_);
      const float rstd = __builtin_amdgcn_rsqf(fmaxf(sST[80 + row] * (1.0f / D_) - mean * mean, 0.f) + 1e-5f);
#pragma unroll
      for (int i = 0; i < 3; ++i) {
        u32x2 v2;
        v2[0] = pack_bf16((acc[m][i][0] - mean * cs[i][0]) * rstd, (acc[m][i][1] - mean * cs[i][1]) * rstd);
        v2[1] = pack_bf16((acc[m][i][2] - mean * cs[i][2]) * rstd, (acc[m][i][3] - mean * cs[i][3]) * rstd);
        *(u32x2*)(sQ + row * K1_QP + ((3 * nq + i) * 16 + 4 * g) * 2) = v2;
      }
    }
  }
  WAIT_LGKM0();
  BAR();

  const int mt = w & 3, nh = w >> 2;
  {
    const int hd = nh;
    const int t0row = 16 * mt;
    u32x4 qf;
    {
      const char* qp = sQ + (t0row + 16 + row16) * K1_QP + hd * 56 + g * 16;
      u32x2 a0 = *(const u32x2*)qp, a1 = *(const u32x2*)(qp + 8);
      qf[0] = a0[0]; qf[1] = a0[1]; qf[2] = g == 3 ? 0u : a1[0]; qf[3] = g == 3 ? 0u : a1[1];
    }
    f32x4 sc[2];
#pragma unroll
    for (int kt = 0; kt < 2; ++kt) {
      const char* kp = sQ + (t0row + 16 * kt + row16) * K1_QP + 128 + hd * 56 + g * 16;
      u32x2 a0 = *(const u32x2*)kp, a1 = *(const u32x2*)(kp + 8);
      u32x4 kf; kf[0] = a0[0]; kf[1] = a0[1]; kf[2] = g == 3 ? 0u : a1[0]; kf[3] = g == 3 ? 0u : a1[1];
      sc[kt] = mfma(__builtin_bit_cast(bf16x8, kf), qf, (f32x4){0.f, 0.f, 0.f, 0.f});
    }
    float lg[8];
    float mx = -3.0e38f;
#pragma unroll
    for (int kt = 0; kt < 2; ++kt)
#pragma unroll
      for (int rr = 0; rr < 4; ++rr) {
        int dist = 16 + row16 - (16 * kt + 4 * g + rr);
        bool inwin = dist >= 0 && dist <= 14 && !(dist & 1);
        float d = rbf(rbf(sc[kt][rr]) * 0.18898223650461362f);
        d = (m0 + 16 * mt + row16 - dist >= 0) ? d : -1e9f;
        lg[kt * 4 + rr] = inwin ? d : -3.0e38f;
        mx = fmaxf(mx, lg[kt * 4 + rr]);
      }
    mx = fmaxf(mx, __shfl_xor(mx, 16)); mx = fmaxf(mx, __shfl_xor(mx, 32));
    float e[8], sm = 0.f;
#pragma unroll
    for (int i = 0; i < 8; ++i) { e[i] = lg[i] > -2.0e38f ? __expf(lg[i] - mx) : 0.f; sm += e[i]; }
    sm += __shfl_xor(sm, 16); sm += __shfl_xor(sm, 32);
    const float inv = __builtin_amdgcn_rcpf(sm);
    u32x4 pf4;
#pragma unroll
    for (int i = 0; i < 4; ++i) pf4[i] = pack_bf16(e[2 * i] * inv, e[2 * i + 1] * inv);
#pragma unroll
    for (int dt = 0; dt < 2; ++dt) {
      const char* vp = sQ + (t0row + 4 * g + q) * K1_QP + 256 + hd * 56 + dt * 32 + p4 * 8;
      s16x4 va = __builtin_amdgcn_ds_read_tr16_b64_v4i16((LDS_AS s16x4*)(vp));
      s16x4 vb = __builtin_amdgcn_ds_read_tr16_b64_v4i16((LDS_AS s16x4*)(vp + 16 * K1_QP));
      s16x8 vv = __builtin_shufflevector(va, vb, 0, 1, 2, 3, 4, 5, 6, 7);
      f32x4 oo = mfma(__builtin_bit_cast(bf16x8, vv), pf4, (f32x4){0.f, 0.f, 0.f, 0.f});
      if (dt == 0 || g < 3) {
        u32x2 s0; s0[0] = pack_bf16(oo[0], oo[1]); s0[1] = pack_bf16(oo[2], oo[3]);
        *(u32x2*)(o + (size_t)(m0 + 16 * mt + row16) * D_ + hp * 56 + hd * 28 + dt * 16 + 4 * g) = s0;
      }
    }
  }
  WAIT_VMN(0);
  PIN(pf[0]); PIN(pf[1]); PIN(pf[2]);
}

template <int NCH, int NST, int SUB, bool STATS>
__global__ __launch_bounds__(768) void gemm64(const u16* __restrict__ A, const u16* __restrict__ B,
                                              const u16* __restrict__ resid, u16* __restrict__ out,
                                              float2* __restrict__ stats, const u16* __restrict__ wg,
                                              const u16* __restrict__ wu, float* __restrict__ colsum) {
  extern __shared__ __attribute__((aligned(16))) char smem[];
  constexpr int LDA = NCH * SUB * 64;
  constexpr int STG = SUB * 16384;
  const int tid = threadIdx.x, l = tid & 63;
  const int w = __builtin_amdgcn_readfirstlane(tid >> 6);
  const int bid = blockIdx.x;
  if (STATS && bid >= 224) {
    const int j = bid - 224;
    if (j < 28 && tid < 512) {
      const u16* W = (j < 14 ? wg : wu) + (j % 14) * 128;
      const int cc = tid & 15, rs = tid >> 4;
      float a[8];
#pragma unroll
      for (int i = 0; i < 8; ++i) a[i] = 0.f;
#pragma unroll
      for (int i = 0; i < 14; ++i) {
        u32x4 v = *(const u32x4*)(W + (size_t)(rs + 32 * i) * F_ + cc * 8);
#pragma unroll
        for (int e = 0; e < 4; ++e) { a[2 * e] += bf_lo(v[e]); a[2 * e + 1] += bf_hi(v[e]); }
      }
      float* red = (float*)smem;
#pragma unroll
      for (int i = 0; i < 8; ++i) red[rs * 128 + cc * 8 + i] = a[i];
      __syncthreads();
      if (tid < 128) {
        float s = 0.f;
#pragma unroll
        for (int i = 0; i < 32; ++i) s += red[i * 128 + tid];
        colsum[j * 128 + tid] = s;
      }
    }
    return;
  }
  const int loc = bid >> 3;
  const int mblk = (bid & 7) * 4 + (loc & 3), nblk = loc >> 2;
  const int m0 = mblk * 64, n0 = nblk * 64;
  const int r = l >> 3, c = l & 7;
  const int row16 = l & 15, g = l >> 4, q = (l >> 2) & 3, p4 = l & 3;
  const int mt = w & 3, nh = w >> 2;

  if (w >= 8) {
    const int lw = w - 8;
    const int tp = c >> 1, half = c & 1;
    const unsigned s_off = lds_off(smem);
    const unsigned avo = (unsigned)((m0 + lw * 8 + r) * LDA + ((c ^ ((lw & 1) * 4 + (r >> 1))) * 8)) * 2u;
    const unsigned bvo = (unsigned)((lw * 8 + r) * D_ + n0 + 16 * (tp ^ (((r >> 1) & 1) | ((lw & 1) << 1))) + 8 * half) * 2u;
    auto issue = [&](int ch) {
#pragma unroll
      for (int sub = 0; sub < SUB; ++sub) {
        unsigned dst = s_off + (ch % NST) * STG + sub * 16384;
        const int k0 = (ch * SUB + sub) * 64;
        glds16(A + k0, avo, dst + lw * 1024);
        glds16(A + k0 + 32 * LDA, avo, dst + (4 + lw) * 1024);
        glds16(B + (size_t)k0 * D_, bvo, dst + 8192 + lw * 1024);
        glds16(B + (size_t)(k0 + 32) * D_, bvo, dst + 8192 + (4 + lw) * 1024);
      }
    };
#pragma unroll
    for (int ch = 0; ch < cmin(NST, NCH); ++ch) issue(ch);
#pragma unroll
    for (int b = 0; b < NCH; ++b) {
      WAIT_VMN(4 * SUB * cmin(b == 0 ? NST - 1 : NST - 2, NCH - 1 - b));
      BAR();
      if (b >= 1 && b - 1 + NST < NCH) issue(b - 1 + NST);
    }
    return;
  }
  u32x2 rv[2];
  {
    unsigned voff = (unsigned)((m0 + 16 * mt + row16) * D_ + n0 + nh * 32 + 4 * g) * 2u;
    rv[0] = gload8<0>(resid, voff); rv[1] = gload8<32>(resid, voff);
  }
  constexpr int KQ = 2 * SUB, MT = SUB == 2 ? 4 : 2;
  const int cn = w & 1, kq = (w >> 1) & (KQ - 1), cm = SUB == 2 ? 0 : (w >> 2);
  const int aoff = (kq >> 1) * 16384 + (cm * 4 + (row16 >> 3)) * 1024 + (row16 & 7) * 128 + (((4 * (kq & 1) + g) ^ ((row16 >> 1) & 7)) * 16);
  const int boff0 = (kq >> 1) * 16384 + 8192 + (kq & 1) * 4096 + g * 1024 + q * 128 + ((((q >> 1) & 1) | ((g & 1) << 1)) * 32) + p4 * 8;
  f32x4 pacc[MT][2];
#pragma unroll
  for (int m = 0; m < MT; ++m) { pacc[m][0] = (f32x4){0.f, 0.f, 0.f, 0.f}; pacc[m][1] = pacc[m][0]; }
  struct FS { u32x4 a[MT]; bf16x8 b[2]; };
  auto rd = [&](FS& f, int ch) {
    const char* st = smem + (ch % NST) * STG;
#pragma unroll
    for (int t = 0; t < 2; ++t) f.b[t] = ldB(st + (boff0 ^ ((2 * cn + t) << 5)));
#pragma unroll
    for (int m = 0; m < MT; ++m) f.a[m] = ldA(st + aoff + m * 2048);
  };
  auto mm = [&](const FS& f) {
#pragma unroll
    for (int m = 0; m < MT; ++m)
#pragma unroll
      for (int t = 0; t < 2; ++t) pacc[m][t] = mfma(f.b[t], f.a[m], pacc[m][t]);
  };
  FS f0, f1;
  BAR();
  rd(f0, 0);
#pragma unroll 1
  for (int ch = 0; ch + 2 < NCH; ch += 2) {
    WAIT_LGKM0(); BAR(); rd(f1, ch + 1);
    mm(f0);
    __builtin_amdgcn_sched_barrier(0);
    WAIT_LGKM0(); BAR(); rd(f0, ch + 2);
    mm(f1);
    __builtin_amdgcn_sched_barrier(0);
  }
  if (NCH % 2 == 0) {
    WAIT_LGKM0(); BAR(); rd(f1, NCH - 1);
    mm(f0);
    __builtin_amdgcn_sched_barrier(0);
    mm(f1);
  } else {
    mm(f0);
  }
  {
    f32x4* red = (f32x4*)smem;
#pragma unroll
    for (int m = 0; m < MT; ++m)
#pragma unroll
      for (int t = 0; t < 2; ++t) red[((kq * 4 + cm * 2 + m) * 4 + 2 * cn + t) * 64 + l] = pacc[m][t];
  }
  WAIT_LGKM0();
  BAR();
  f32x4 acc[2];
  {
    const f32x4* red = (const f32x4*)smem;
#pragma unroll
    for (int t = 0; t < 2; ++t) {
      acc[t] = red[((0 * 4 + mt) * 4 + 2 * nh + t) * 64 + l];
#pragma unroll
      for (int k = 1; k < KQ; ++k) acc[t] += red[((k * 4 + mt) * 4 + 2 * nh + t) * 64 + l];
    }
  }
  WAIT_VMN(0);
  PIN(rv[0]); PIN(rv[1]);
  float hv[2][4];
#pragma unroll
  for (int t = 0; t < 2; ++t) {
    hv[t][0] = rbf(bf_lo(rv[t][0]) + rbf(acc[t][0])); hv[t][1] = rbf(bf_hi(rv[t][0]) + rbf(acc[t][1]));
    hv[t][2] = rbf(bf_lo(rv[t][1]) + rbf(acc[t][2])); hv[t][3] = rbf(bf_hi(rv[t][1]) + rbf(acc[t][3]));
    u32x2 o2; o2[0] = pack_bf16(hv[t][0], hv[t][1]); o2[1] = pack_bf16(hv[t][2], hv[t][3]);
    *(u32x2*)(smem + 65536 + (16 * mt + row16) * 144 + (nh * 32 + 16 * t + 4 * g) * 2) = o2;
  }
  WAIT_LGKM0();
  BAR();
  {
    u32x4 v = *(const u32x4*)(smem + 65536 + (w * 8 + r) * 144 + c * 16);
    gstore16_wt(out + (size_t)(m0 + w * 8 + r) * D_ + n0 + c * 8, v);
  }
  if (STATS) {
    float s = 0.f;
#pragma unroll
    for (int t = 0; t < 2; ++t) s += hv[t][0] + hv[t][1] + hv[t][2] + hv[t][3];
    s += __shfl_xor(s, 16); s += __shfl_xor(s, 32);
    float mean = s * (1.0f / 32.0f), m2 = 0.f;
#pragma unroll
    for (int t = 0; t < 2; ++t)
#pragma unroll
      for (int j = 0; j < 4; ++j) { float d = hv[t][j] - mean; m2 += d * d; }
    m2 += __shfl_xor(m2, 16); m2 += __shfl_xor(m2, 32);
    if (g == 0) stats[(size_t)(m0 + 16 * mt + row16) * 14 + nblk * 2 + nh] = make_float2(s, m2);
  }
}

constexpr int K3_STAGE = 48 * 1024;
constexpr int K3_NST = 3;
constexpr int K3_LDS = K3_NST * K3_STAGE + 2048;

__global__ __launch_bounds__(768) void k3_ffn1(const u16* __restrict__ h, const float2* __restrict__ stats,
                                               const u16* __restrict__ wg, const u16* __restrict__ wu,
                                               const float* __restrict__ colsum, u16* __restrict__ act) {
  extern __shared__ __attribute__((aligned(16))) char smem[];
  float2* sRS = (float2*)(smem + K3_NST * K3_STAGE);
  float* sCSm = (float*)(smem + K3_NST * K3_STAGE + 1024);
  const int tid = threadIdx.x, l = tid & 63;
  const int w = __builtin_amdgcn_readfirstlane(tid >> 6);
  const int bid = blockIdx.x;
  const int xcd = bid & 7, loc = bid >> 3;
  const int mblk = (xcd >> 1) * 4 + (loc & 3), fblk = (xcd & 1) * 7 + (loc >> 2);
  const int m0 = mblk * 128, f0 = fblk * 128;
  const int r = l >> 3, c = l & 7;
  const int row16 = l & 15, g = l >> 4, q = (l >> 2) & 3, p4 = l & 3;
  if (w >= 8) {
    const int lw = w - 8;
    const int tp = c >> 1, half = c & 1;
    const unsigned s_off = lds_off(smem);
    const unsigned avo = (unsigned)((m0 + lw * 8 + r) * D_ + ((c ^ ((lw & 1) * 4 + (r >> 1))) * 8)) * 2u;
    const unsigned bvo = (unsigned)((lw * 8 + r) * F_ + f0 + 16 * (tp ^ (((r >> 1) & 1) | ((lw & 1) << 1))) + 8 * half) * 2u;
    auto issue = [&](int ch) {
      unsigned dst = s_off + (ch % K3_NST) * K3_STAGE;
#pragma unroll
      for (int i = 0; i < 4; ++i) glds16(h + ch * 64 + i * 32 * D_, avo, dst + (4 * i + lw) * 1024);
#pragma unroll
      for (int mc = 0; mc < 4; ++mc)
#pragma unroll
        for (int hk = 0; hk < 2; ++hk) {
          const u16* W = ((mc >> 1) ? wu : wg) + (size_t)(ch * 64 + hk * 32) * F_ + (mc & 1) * 64;
          glds16(W, bvo, dst + (16 + mc * 8 + hk * 4 + lw) * 1024);
        }
    };
#pragma unroll
    for (int ch = 0; ch < K3_NST; ++ch) issue(ch);
#pragma unroll
    for (int b = 0; b < 7; ++b) {
      WAIT_VMN(12 * cmin(b == 0 ? K3_NST - 1 : K3_NST - 2, 6 - b));
      BAR();
      if (b >= 1 && b - 1 + K3_NST < 7) issue(b - 1 + K3_NST);
    }
    return;
  }
  if (tid >= 128 && tid < 384) {
    int i = tid - 128;
    sCSm[i] = colsum[(i >> 7) * F_ + f0 + (i & 127)];
  }
  if (tid < 128) {
    const float2* sp = stats + (size_t)(m0 + tid) * 14;
    float2 p[14];
#pragma unroll
    for (int i = 0; i < 14; ++i) p[i] = sp[i];
    float tot = 0.f;
#pragma unroll
    for (int i = 0; i < 14; ++i) tot += p[i].x;
    float mean = tot * (1.0f / D_), m2 = 0.f;
#pragma unroll
    for (int i = 0; i < 14; ++i) { float dm = p[i].x * (1.0f / 32.0f) - mean; m2 += p[i].y + 32.0f * dm * dm; }
    sRS[tid] = make_float2(mean, __builtin_amdgcn_rsqf(m2 * (1.0f / D_) + 1e-5f));
  }

  const int mh = w & 1, nq = w >> 1;
  const int aoff = (8 * mh + (row16 >> 3)) * 1024 + (row16 & 7) * 128;
  const int aswz = (row16 >> 1) & 7;
  const int boffb = 16384 + (nq >> 1) * 8192 + g * 1024 + q * 128 + ((((q >> 1) & 1) | ((g & 1) << 1)) * 32) + p4 * 8;
  const int boff0 = boffb ^ ((2 * (nq & 1)) << 5), boff1 = boffb ^ ((2 * (nq & 1) + 1) << 5);
  f32x4 acc[4][2][2];
#pragma unroll
  for (int m = 0; m < 4; ++m)
#pragma unroll
    for (int i = 0; i < 2; ++i)
#pragma unroll
      for (int j = 0; j < 2; ++j) acc[m][i][j] = (f32x4){0.f, 0.f, 0.f, 0.f};

  struct FS { u32x4 a[4]; bf16x8 b[2][2]; };
  auto rd = [&](FS& f, int ch, int ks2) {
    const char* stg = smem + (ch % K3_NST) * K3_STAGE;
    int cs = ((4 * ks2 + g) ^ aswz) * 16;
#pragma unroll
    for (int gu = 0; gu < 2; ++gu) {
      f.b[gu][0] = ldB(stg + gu * 16384 + ks2 * 4096 + boff0);
      f.b[gu][1] = ldB(stg + gu * 16384 + ks2 * 4096 + boff1);
    }
#pragma unroll
    for (int m = 0; m < 4; ++m) f.a[m] = ldA(stg + aoff + m * 2048 + cs);
  };
  auto mm = [&](const FS& f) {
#pragma unroll
    for (int m = 0; m < 4; ++m)
#pragma unroll
      for (int gu = 0; gu < 2; ++gu)
#pragma unroll
        for (int t = 0; t < 2; ++t) acc[m][gu][t] = mfma(f.b[gu][t], f.a[m], acc[m][gu][t]);
  };
  FS fa, fb;
  WAIT_LGKM0();
  BAR();
  rd(fa, 0, 0);
#pragma unroll 1
  for (int ch = 0; ch < 6; ++ch) {
    WAIT_LGKM0();
    rd(fb, ch, 1);
    mm(fa);
    ILV(12);
    __builtin_amdgcn_sched_barrier(0);
    WAIT_LGKM0();
    BAR();
    rd(fa, ch + 1, 0);
    mm(fb);
    ILV(12);
    __builtin_amdgcn_sched_barrier(0);
  }
  WAIT_LGKM0();
  rd(fb, 6, 1);
  mm(fa);
  ILV(12);
  __builtin_amdgcn_sched_barrier(0);
  f32x4 cg[2], cu[2];
  float2 rs[4];
#pragma unroll
  for (int t = 0; t < 2; ++t) {
    cg[t] = *(const f32x4*)(sCSm + nq * 32 + 16 * t + 4 * g);
    cu[t] = *(const f32x4*)(sCSm + 128 + nq * 32 + 16 * t + 4 * g);
  }
#pragma unroll
  for (int m = 0; m < 4; ++m) rs[m] = sRS[64 * mh + 16 * m + row16];
  mm(fb);
  {
    float gv[4][2][4], uv[4][2][4], ex[4][2][4];
#pragma unroll
    for (int m = 0; m < 4; ++m)
#pragma unroll
      for (int t = 0; t < 2; ++t)
#pragma unroll
        for (int j = 0; j < 4; ++j) {
          gv[m][t][j] = (acc[m][0][t][j] - rs[m].x * cg[t][j]) * rs[m].y;
          uv[m][t][j] = (acc[m][1][t][j] - rs[m].x * cu[t][j]) * rs[m].y;
        }
    __builtin_amdgcn_sched_barrier(0);
#pragma unroll
    for (int m = 0; m < 4; ++m)
#pragma unroll
      for (int t = 0; t < 2; ++t)
#pragma unroll
        for (int j = 0; j < 4; ++j) ex[m][t][j] = __expf(-gv[m][t][j]);
    __builtin_amdgcn_sched_barrier(0);
#pragma unroll
    for (int m = 0; m < 4; ++m)
#pragma unroll
      for (int t = 0; t < 2; ++t)
#pragma unroll
        for (int j = 0; j < 4; ++j) ex[m][t][j] = __builtin_amdgcn_rcpf(1.0f + ex[m][t][j]);
    __builtin_amdgcn_sched_barrier(0);
#pragma unroll
    for (int m = 0; m < 4; ++m)
#pragma unroll
      for (int t = 0; t < 2; ++t) {
        float a4[4];
#pragma unroll
        for (int j = 0; j < 4; ++j) a4[j] = (gv[m][t][j] * ex[m][t][j]) * uv[m][t][j];
        u32x2 o2; o2[0] = pack_bf16(a4[0], a4[1]); o2[1] = pack_bf16(a4[2], a4[3]);
        *(u32x2*)(smem + K3_STAGE + (64 * mh + 16 * m + row16) * 272 + (nq * 32 + 16 * t + 4 * g) * 2) = o2;
      }
  }
  WAIT_LGKM0();
  BAR();
#pragma unroll
  for (int i = 0; i < 4; ++i) {
    int row = (w * 4 + i) * 4 + (l >> 4);
    u32x4 v = *(const u32x4*)(smem + K3_STAGE + row * 272 + (l & 15) * 16);
    gstore16_wt(act + (size_t)(m0 + row) * F_ + f0 + (l & 15) * 8, v);
  }
}

constexpr int K2_NST = 8, K4_NST = 5;
#define K2_KERN gemm64<7, K2_NST, 1, true>
#define K4_KERN gemm64<14, K4_NST, 2, false>
constexpr int K2_LDS = K2_NST * 16384, K4_LDS = K4_NST * 32768;

#define NKERN 4
static void launch_one(int k, void* const* d_in, void* d_out, void* d_ws, hipStream_t stream) {
  const u16* x = (const u16*)d_in[0];
  const u16* wqkv = (const u16*)d_in[1];
  const u16* wo = (const u16*)d_in[2];
  const u16* wgate = (const u16*)d_in[4];
  const u16* wup = (const u16*)d_in[5];
  const u16* wdown = (const u16*)d_in[6];
  u16* out = (u16*)d_out;
  char* ws = (char*)d_ws;
  u16* o = (u16*)ws;
  u16* h = (u16*)(ws + (2u << 20));
  float2* stats = (float2*)(ws + (4u << 20));
  float* colsum = (float*)(ws + (6u << 20));
  u16* act = (u16*)(ws + (8u << 20));
  if (k == 0) k1_qkv_attn<<<256, 768, K1_LDS, stream>>>(x, wqkv, o, wo, wgate, wup, wdown);
  if (k == 1) K2_KERN<<<256, 768, K2_LDS, stream>>>(o, wo, x, h, stats, wgate, wup, colsum);
  if (k == 2) k3_ffn1<<<224, 768, K3_LDS, stream>>>(h, stats, wgate, wup, colsum, act);
  if (k == 3) K4_KERN<<<224, 768, K4_LDS, stream>>>(act, wdown, h, out, nullptr, nullptr, nullptr, nullptr);
}

extern "C" void kernel_launch(void* const* d_in, const int* in_sizes, int n_in, void* d_out, int out_size,
                              void* d_ws, size_t ws_size, hipStream_t stream) {
  hipFuncSetAttribute((const void*)k1_qkv_attn, hipFuncAttributeMaxDynamicSharedMemorySize, K1_LDS);
  hipFuncSetAttribute((const void*)K2_KERN, hipFuncAttributeMaxDynamicSharedMemorySize, K2_LDS);
  hipFuncSetAttribute((const void*)K4_KERN, hipFuncAttributeMaxDynamicSharedMemorySize, K4_LDS);
  hipFuncSetAttribute((const void*)k3_ffn1, hipFuncAttributeMaxDynamicSharedMemorySize, K3_LDS);
  for (int k = 0; k < NKERN; ++k) launch_one(k, d_in, d_out, d_ws, stream);
}
```
